```python
import torch

def _softmax(x, axis=-1):
    return torch.softmax(x, dim=axis)
def _gelu(x):
    return torch.nn.functional.gelu(x, approximate="tanh")
def _silu(x):
    return torch.nn.functional.silu(x)
def _sigmoid(x):
    return torch.sigmoid(x)
def _layernorm(x, axis=-1, eps=1e-5):
    m = x.mean(dim=axis, keepdim=True)
    v = x.var(dim=axis, keepdim=True, unbiased=False)
    return (x - m) / torch.sqrt(v + eps)
def _scatter_add(tgt, idx, src, axis=0):
    return tgt.clone().index_add_(axis, idx, src)
def _segment_sum(src, seg, n):
    out = torch.zeros((n,) + src.shape[1:], dtype=src.dtype)
    return out.index_add_(0, seg, src)
def _pad(x, pw):
    flat = tuple(v for pair in reversed(pw) for v in pair)
    return torch.nn.functional.pad(x, flat)

kernel_name = 'gated_mix__gqa_block_cached__moe_arch_mod__decode'

def setup_inputs():

    _w = (lambda a: (lambda t: t.to(torch.bfloat16) if t.is_floating_point() else t)(torch.as_tensor(a)))
    def _A_setup():
        B, Sq, D = 1, 1, 1024
        Hq, Hkv, Sk, Dh = 32, 4, 65536, 128
        assert Hq % Hkv == 0, (Hq, Hkv)
        return {
            "x": _w((lambda *s: torch.randn(*s, dtype=torch.bfloat16) * 0.05)(B, Sq, D)),
            "k_cache": _w((lambda *s: torch.randn(*s, dtype=torch.bfloat16) * 3.2)(B, Hkv, Sk, Dh)),
            "v_cache": _w((lambda *s: torch.randn(*s, dtype=torch.bfloat16) * 1.6)(B, Hkv, Sk, Dh)),
            "wq": _w((lambda *s: torch.randn(*s, dtype=torch.bfloat16) * 3.2)(D, Hq * Dh) * (D ** -0.5)),
            "wo": _w((lambda *s: torch.randn(*s, dtype=torch.bfloat16) * 1.6)(Hq * Dh, D) * ((Hq * Dh) ** -0.5)),
        }
    _A_in = _A_setup()

    def _B_setup():
        B, S, D, F, C = 1, 1, 1024, 3584, 1
        return {
            "x": _w((lambda *s: torch.randn(*s, dtype=torch.bfloat16) * 0.05)(B, S, D)),
            "w_r": _w((lambda *s: torch.randn(*s, dtype=torch.bfloat16) * 0.05)(D)),
            "w_gate": _w((lambda *s: torch.randn(*s, dtype=torch.bfloat16) * 0.05)(D, F)),
            "w_up": _w((lambda *s: torch.randn(*s, dtype=torch.bfloat16) * 0.05)(D, F)),
            "w_down": _w((lambda *s: torch.randn(*s, dtype=torch.bfloat16) * 0.05)(F, D)),
        }
    _B_in = _B_setup()

    return {
        "A_x": _A_in["x"],
        "A_k_cache": _A_in["k_cache"],
        "A_v_cache": _A_in["v_cache"],
        "A_wq": _A_in["wq"],
        "A_wo": _A_in["wo"],
        "B_x": _B_in["x"],
        "B_w_r": _B_in["w_r"],
        "B_w_gate": _B_in["w_gate"],
        "B_w_up": _B_in["w_up"],
        "B_w_down": _B_in["w_down"],
        "gate_w": _w((lambda *s: torch.randn(*s, dtype=torch.bfloat16) * 0.05)(1024) * 16.0),
    }

def reference(A_x, A_k_cache, A_v_cache, A_wq, A_wo, B_x, B_w_r, B_w_gate, B_w_up, B_w_down, gate_w):
    def _A_ref(x, k_cache, v_cache, wq, wo):
        B, Sq, D = x.shape
        Hq, Dh = 32, 128
        Hkv = k_cache.shape[1]
        rep = Hq // Hkv
        q = (lambda x, *d: x.permute(*d))((x @ wq).reshape(B, Sq, Hq, Dh), 0, 2, 1, 3)
        k = (lambda x, n, axis: torch.repeat_interleave(x, n, dim=axis))(k_cache, rep, axis=1)
        v = (lambda x, n, axis: torch.repeat_interleave(x, n, dim=axis))(v_cache, rep, axis=1)
        scores = torch.einsum("bhqd,bhkd->bhqk", q, k) * (Dh ** -0.5)
        p = _softmax(scores, axis=-1)
        o = torch.einsum("bhqk,bhkd->bhqd", p, v)
        return (lambda x, *d: x.permute(*d))(o, 0, 2, 1, 3).reshape(B, Sq, Hq * Dh) @ wo

    def _B_ref(x, w_r, w_gate, w_up, w_down):
        B, S, D = x.shape
        C = 1
        r = x @ w_r
        idx = torch.argsort(-r, axis=-1)[:, :C]
        idx_e = (lambda x, n, axis: torch.repeat_interleave(x, n, dim=axis))(idx.reshape(B, C, 1), D, axis=2)
        x_sel = (lambda x, i, axis: torch.take_along_dim(x, i, dim=axis))(x, idx_e, axis=1)
        g = _silu(x_sel @ w_gate)
        u = x_sel @ w_up
        y = (g * u) @ w_down
        r_sel = _sigmoid((lambda x, i, axis: torch.take_along_dim(x, i, dim=axis))(r, idx, axis=1))
        y = y * r_sel.reshape(B, C, 1)
        b_off = (torch.arange(B) * S).reshape(B, 1)
        flat_idx = (idx + b_off).reshape(B * C)
        zero = (x * 0.0).reshape(B * S, D)
        upd = _scatter_add(zero, flat_idx, y.reshape(B * C, D), axis=0)
        return x + upd.reshape(B, S, D)

    x = A_x
    g = _sigmoid((x * gate_w).sum(-1, keepdims=True))
    a = _A_ref(x, A_k_cache, A_v_cache, A_wq, A_wo)
    b = _B_ref(x, B_w_r, B_w_gate, B_w_up, B_w_down)
    return g * a + (1 - g) * b
```

```cpp
#include <hip/hip_runtime.h>
#include <hip/hip_bf16.h>
#include <stdint.h>

#define GAS __attribute__((address_space(1)))
#define LAS __attribute__((address_space(3)))

typedef unsigned v4u __attribute__((ext_vector_type(4)));
typedef unsigned v2u __attribute__((ext_vector_type(2)));
typedef float f32x4 __attribute__((ext_vector_type(4)));
typedef float f32x2 __attribute__((ext_vector_type(2)));
typedef short bf16x8 __attribute__((ext_vector_type(8)));
typedef short bf16x4 __attribute__((ext_vector_type(4)));
typedef __bf16 bf2 __attribute__((ext_vector_type(2)));

namespace {

constexpr int NWG = 256, NSW = 8, NTHR = (NSW + 1) * 64;
constexpr int DM = 1024, SK = 65536, DH = 128, FF = 3584, NQ = 4096;
constexpr int NT = 32;
#define RING 8
constexpr int R = RING;

constexpr int WS_EPOCH = 0;
constexpr int WS_Q = 4096;
constexpr int X1_STRIDE = 64 * 128 + 64 * 16;
constexpr int WS_X1 = WS_Q + 2048 * 8;
constexpr int WS_X2 = WS_X1 + NWG * X1_STRIDE;
constexpr int X2_STRIDE = NWG * 32;
constexpr int WS_END = WS_X2 + NWG * X2_STRIDE;

constexpr int L_FLAGS = 0;
constexpr int L_SC = 64;
constexpr int L_HV = 128;
constexpr int L_OV = 192;
constexpr int L_QP = 256;
constexpr int L_GU = L_QP + 512;
constexpr int L_QF = L_GU + 1024;
constexpr int L_ML = L_QF + 2048;
constexpr int L_MRG = L_ML + 512;
constexpr int L_END = L_MRG + 8 * 8 * 128 * 4;
constexpr int LDS_BYTES = 96 * 1024;
static_assert(L_END <= LDS_BYTES, "lds");

constexpr unsigned SPIN_CAP = 1u << 18;
constexpr int AUX_SC1 = 16;

struct Params {
    const unsigned short* x;
    const unsigned short* kc;
    const unsigned short* vc;
    const unsigned short* wq;
    const unsigned short* wo;
    const unsigned short* wr;
    const unsigned short* wg;
    const unsigned short* wu;
    const unsigned short* wd;
    const unsigned short* gw;
    unsigned short* out;
    unsigned char* ws;
};

__device__ __forceinline__ v4u ldnt(const unsigned char* p) { return __builtin_nontemporal_load((const GAS v4u*)p); }
__device__ __forceinline__ float bflo(unsigned u) { return __builtin_bit_cast(float, u << 16); }
__device__ __forceinline__ float bfhi(unsigned u) { return __builtin_bit_cast(float, u & 0xffff0000u); }
__device__ __forceinline__ unsigned pk(float lo, float hi) {
    bf2 r = {(__bf16)lo, (__bf16)hi};
    return __builtin_bit_cast(unsigned, r);
}
__device__ __forceinline__ float rbf(float v) { return bflo(pk(v, v)); }
__device__ __forceinline__ unsigned fu(float f) { return __builtin_bit_cast(unsigned, f); }
__device__ __forceinline__ float uf(unsigned u) { return __builtin_bit_cast(float, u); }
template <int CTRL> __device__ __forceinline__ float dpp(float x) {
    return uf((unsigned)__builtin_amdgcn_mov_dpp((int)fu(x), CTRL, 0xf, 0xf, true));
}
constexpr int XOR1 = 0xB1, XOR2 = 0x4E, XOR7 = 0x141, XOR8 = 0x128, ROR4 = 0x124;
__device__ __forceinline__ float swap32_add(float a, float b) {
    auto s = __builtin_amdgcn_permlane32_swap(fu(a), fu(b), false, false);
    return uf(s[0]) + uf(s[1]);
}
__device__ __forceinline__ float swap16_add(float a, float b) {
    auto s = __builtin_amdgcn_permlane16_swap(fu(a), fu(b), false, false);
    return uf(s[0]) + uf(s[1]);
}
__device__ __forceinline__ float x16x32_max(float x) {
    auto s = __builtin_amdgcn_permlane16_swap(fu(x), fu(x), false, false);
    x = fmaxf(uf(s[0]), uf(s[1]));
    auto t = __builtin_amdgcn_permlane32_swap(fu(x), fu(x), false, false);
    return fmaxf(uf(t[0]), uf(t[1]));
}
__device__ __forceinline__ float x16x32_sum(float x) { x = swap16_add(x, x); return swap32_add(x, x); }
__device__ __forceinline__ float row_sum(float x) {
    x += dpp<XOR1>(x); x += dpp<XOR2>(x); x += dpp<XOR7>(x); x += dpp<XOR8>(x); return x;
}
__device__ __forceinline__ float row_max(float x) {
    x = fmaxf(x, dpp<XOR1>(x)); x = fmaxf(x, dpp<XOR2>(x)); x = fmaxf(x, dpp<XOR7>(x)); x = fmaxf(x, dpp<XOR8>(x)); return x;
}
__device__ __forceinline__ float wave_sum(float x) { x = row_sum(x); x = swap16_add(x, x); return swap32_add(x, x); }
__device__ __forceinline__ float wave_max(float x) { return x16x32_max(row_max(x)); }

__device__ __forceinline__ f32x4 mfma32(v4u a, v4u b, f32x4 c) {
    return __builtin_amdgcn_mfma_f32_16x16x32_bf16(__builtin_bit_cast(bf16x8, a), __builtin_bit_cast(bf16x8, b), c, 0, 0, 0);
}
__device__ __forceinline__ f32x4 mfma16(v2u a, v2u b, f32x4 c) {
    return __builtin_amdgcn_mfma_f32_16x16x16bf16_1k(__builtin_bit_cast(bf16x4, a), __builtin_bit_cast(bf16x4, b), c, 0, 0, 0);
}
#define WG_BARRIER() do { asm volatile("s_waitcnt lgkmcnt(0)" ::: "memory"); __builtin_amdgcn_s_barrier(); asm volatile("" ::: "memory"); } while (0)
#define SB() __builtin_amdgcn_sched_barrier(0)

__device__ __forceinline__ void fma8(float (&acc)[8], v4u wv, float xv) {
    acc[0] = fmaf(xv, bflo(wv.x), acc[0]); acc[1] = fmaf(xv, bfhi(wv.x), acc[1]);
    acc[2] = fmaf(xv, bflo(wv.y), acc[2]); acc[3] = fmaf(xv, bfhi(wv.y), acc[3]);
    acc[4] = fmaf(xv, bflo(wv.z), acc[4]); acc[5] = fmaf(xv, bfhi(wv.z), acc[5]);
    acc[6] = fmaf(xv, bflo(wv.w), acc[6]); acc[7] = fmaf(xv, bfhi(wv.w), acc[7]);
}
__device__ __forceinline__ f32x2 reduce_cols(const float (&a)[8]) {
    const float b0 = swap32_add(a[0], a[4]), b1 = swap32_add(a[1], a[5]), b2 = swap32_add(a[2], a[6]), b3 = swap32_add(a[3], a[7]);
    float c0 = swap16_add(b0, b2), c1 = swap16_add(b1, b3);
    c0 += dpp<XOR2>(c0); c1 += dpp<XOR2>(c1);
    c0 += dpp<ROR4>(c0); c1 += dpp<ROR4>(c1);
    c0 += dpp<XOR8>(c0); c1 += dpp<XOR8>(c1);
    return f32x2{c0, c1};
}

__device__ __forceinline__ v4u ld_sc1(__amdgpu_buffer_rsrc_t rs, int off) { return __builtin_amdgcn_raw_buffer_load_b128(rs, off, 0, AUX_SC1); }
__device__ __forceinline__ void st_sc1(__amdgpu_buffer_rsrc_t rs, int off, v4u v) { __builtin_amdgcn_raw_buffer_store_b128(v, rs, off, 0, AUX_SC1); }

__device__ __forceinline__ void rows_mfma(f32x4 (&acc)[2], v4u wv, unsigned pp, unsigned m0, unsigned m1) {
    const v2u a = {pp & m0, pp & m1};
    acc[0] = mfma16(a, v2u{wv.x, wv.y}, acc[0]);
    acc[1] = mfma16(a, v2u{wv.z, wv.w}, acc[1]);
}

}

__global__ void __launch_bounds__(NTHR, 1) fused_decode_kernel(Params p) {
    extern __shared__ __attribute__((aligned(16))) unsigned char smem[];
    LAS unsigned char* L = (LAS unsigned char*)smem;
    volatile LAS unsigned* flags = (volatile LAS unsigned*)(L + L_FLAGS);
    const int tid = threadIdx.x, lane = tid & 63;
    const int wv = __builtin_amdgcn_readfirstlane(tid >> 6);
    const int b = blockIdx.x;
    const int w = (b & 7) * 32 + (b >> 3);
    const int kvh = w >> 6, sp = w & 63;
    const int jx = w & 31;
    const bool has_mlp = jx < 28;
    const int mc = (w >> 5) * 28 + jx;
    const __amdgpu_buffer_rsrc_t rs = __builtin_amdgcn_make_buffer_rsrc((void*)p.ws, 0, WS_END, 0x00020000);

    if (wv == NSW) {
        const unsigned tag = __builtin_amdgcn_readfirstlane(__builtin_amdgcn_raw_buffer_load_b32(rs, WS_EPOCH, 0, AUX_SC1)) + 1u;
        if (lane < 3) flags[lane] = lane == 2 ? tag : 0u;
        {
            const GAS v4u* xp = (const GAS v4u*)p.x; const GAS v4u* gp = (const GAS v4u*)p.gw; const GAS v4u* rp = (const GAS v4u*)p.wr;
            float sg = 0.f, sr = 0.f;
#pragma unroll
            for (int i = 0; i < 2; ++i) {
                const v4u xv = xp[lane + 64 * i], gv = gp[lane + 64 * i], rv = rp[lane + 64 * i];
#pragma unroll
                for (int k = 0; k < 4; ++k) {
                    sg += rbf(bflo(xv[k]) * bflo(gv[k])); sg += rbf(bfhi(xv[k]) * bfhi(gv[k]));
                    sr = fmaf(bflo(xv[k]), bflo(rv[k]), sr); sr = fmaf(bfhi(xv[k]), bfhi(rv[k]), sr);
                }
            }
            sg = rbf(wave_sum(sg)); sr = rbf(wave_sum(sr));
            const float g = rbf(1.f / (1.f + __expf(-sg))), rsg = rbf(1.f / (1.f + __expf(-sr)));
            if (lane == 0) { ((LAS float*)(L + L_SC))[0] = g; ((LAS float*)(L + L_SC))[1] = rsg; }
        }
        WG_BARRIER();
        if (lane < 4) {
            f32x4 s = {0.f, 0.f, 0.f, 0.f};
#pragma unroll
            for (int i = 0; i < NSW; ++i) s += *(const LAS f32x4*)(L + L_QP + i * 64 + lane * 16);
            st_sc1(rs, WS_Q + (w * 8 + lane * 2) * 8, v4u{pk(s[0], s[1]), tag, pk(s[2], s[3]), tag});
        }
        bool dead = false;
        {
            const int qoff = WS_Q + (kvh * 512 + ((lane >> 2) & 3) * 64 + (4 * (lane >> 4) + (lane & 3)) * 4) * 8;
            v4u g0, g1, g2, g3;
            for (unsigned spins = 0;; ++spins) {
                asm volatile("" ::: "memory");
                g0 = ld_sc1(rs, qoff); g1 = ld_sc1(rs, qoff + 16); g2 = ld_sc1(rs, qoff + 2048); g3 = ld_sc1(rs, qoff + 2048 + 16);
                const bool ok = g0.y == tag && g0.w == tag && g1.y == tag && g1.w == tag && g2.y == tag && g2.w == tag && g3.y == tag && g3.w == tag;
                if (__all(ok)) break;
                if (spins > SPIN_CAP) { dead = true; break; }
                __builtin_amdgcn_s_sleep(1);
            }
            *(LAS v4u*)(L + L_QF + lane * 16) = v4u{g0.x, g0.z, g1.x, g1.z};
            *(LAS v4u*)(L + L_QF + 1024 + lane * 16) = v4u{g2.x, g2.z, g3.x, g3.z};
            asm volatile("s_waitcnt lgkmcnt(0)" ::: "memory");
            if (lane == 0) flags[0] = 1u;
        }
        WG_BARRIER();
        {
            const int xo = WS_X1 + w * X1_STRIDE;
            v4u d[8], ml;
            for (unsigned spins = 0;; ++spins) {
                asm volatile("" ::: "memory");
                ml = ld_sc1(rs, xo + 8192 + lane * 16);
#pragma unroll
                for (int i = 0; i < 8; ++i) d[i] = ld_sc1(rs, xo + i * 1024 + lane * 16);
                bool ok = ml.y == tag && ml.w == tag;
#pragma unroll
                for (int i = 0; i < 8; ++i) ok = ok && d[i].y == tag && d[i].w == tag;
                if (__all(ok) || dead) break;
                if (spins > SPIN_CAP) { dead = true; break; }
                __builtin_amdgcn_s_sleep(1);
            }
            const float m = uf(ml.x), l = uf(ml.z);
            const float M = wave_max(m);
            const float wt = __builtin_amdgcn_exp2f(m - M);
            const float Lsum = wave_sum(wt * l);
            float o0 = 0.f, o1 = 0.f;
#pragma unroll
            for (int i = 0; i < 8; ++i) {
                const float wi = __shfl(wt, 8 * i + (lane >> 3));
                o0 = fmaf(wi, uf(d[i].x), o0); o1 = fmaf(wi, uf(d[i].z), o1);
            }
            o0 += dpp<XOR8>(o0); o1 += dpp<XOR8>(o1);
            o0 = x16x32_sum(o0); o1 = x16x32_sum(o1);
            const float g = ((const LAS float*)(L + L_SC))[0];
            const float inv = 1.f / Lsum;
            o0 = rbf(o0 * inv) * g; o1 = rbf(o1 * inv) * g;
            if (lane < 8) {
                const unsigned u0 = pk(o0, o0), u1 = pk(o1, o1);
                *(LAS v2u*)(L + L_OV + lane * 8) = v2u{u0, u1};
            }
        }
        WG_BARRIER();
        {
            const int xo = WS_X2 + w * X2_STRIDE;
            v4u d[8];
            for (unsigned spins = 0;; ++spins) {
                asm volatile("" ::: "memory");
#pragma unroll
                for (int i = 0; i < 8; ++i) d[i] = ld_sc1(rs, xo + i * 1024 + lane * 16);
                bool ok = true;
#pragma unroll
                for (int i = 0; i < 8; ++i) ok = ok && d[i].y == tag && d[i].w == tag;
                if (__all(ok) || dead) break;
                if (spins > SPIN_CAP) { dead = true; break; }
                __builtin_amdgcn_s_sleep(1);
            }
            float o0 = 0.f, o1 = 0.f;
#pragma unroll
            for (int i = 0; i < 8; ++i) { o0 += uf(d[i].x); o1 += uf(d[i].z); }
            o0 += dpp<XOR2>(o0); o1 += dpp<XOR2>(o1);
            o0 += dpp<ROR4>(o0); o1 += dpp<ROR4>(o1);
            o0 += dpp<XOR8>(o0); o1 += dpp<XOR8>(o1);
            o0 = x16x32_sum(o0); o1 = x16x32_sum(o1);
            if (lane < 2) {
                const float g = ((const LAS float*)(L + L_SC))[0];
                const unsigned xv = *(const GAS unsigned*)(p.x + 4 * w + 2 * lane);
                const float r0 = fmaf(1.f - g, bflo(xv), o0), r1 = fmaf(1.f - g, bfhi(xv), o1);
                unsigned res = pk(r0, r1);
                if (dead) res = 0x7fc07fc0u;
                *(GAS unsigned*)(p.out + 4 * w + 2 * lane) = res;
            }
            if (w == 0 && lane == 0) __builtin_amdgcn_raw_buffer_store_b32(tag, rs, WS_EPOCH, 0, AUX_SC1);
        }
        return;
    }

    const int row0 = 128 * wv + 4 * (lane >> 1);
    const unsigned char* wq_p = (const unsigned char*)p.wq + (size_t)row0 * (NQ * 2) + w * 32 + (lane & 1) * 16;
    const size_t kv_base = ((size_t)kvh * SK + (size_t)sp * 1024 + wv * 4) * 256;
    const unsigned char* k_p = (const unsigned char*)p.kc + kv_base + ((lane >> 2) & 3) * 256 + (4 * (lane >> 4) + (lane & 3)) * 16;
    const unsigned char* v_p = (const unsigned char*)p.vc + kv_base + lane * 16;
    const unsigned char* wg_p = (const unsigned char*)p.wg + (size_t)row0 * (FF * 2) + mc * 32 + (lane & 1) * 16;
    const unsigned char* wu_p = (const unsigned char*)p.wu + (size_t)row0 * (FF * 2) + mc * 32 + (lane & 1) * 16;
    const unsigned char* wd_p = (const unsigned char*)p.wd + (size_t)(mc * 16 + (lane >> 4)) * (DM * 2) + wv * 256 + (lane & 15) * 16;
    const unsigned char* wo_p = (const unsigned char*)p.wo + (size_t)(w * 16 + (lane >> 4)) * (DM * 2) + wv * 256 + (lane & 15) * 16;

    auto issue = [&](int i) -> v4u {
        if (i < 4) return ldnt(wq_p + (size_t)i * (NQ * 2));
        if (i < 36) return ldnt(k_p + (size_t)(i - 4) * 8192);
        if (i < 68) return ldnt(v_p + (size_t)(i - 36) * 8192);
        if (i < 72) return has_mlp ? ldnt(wg_p + (size_t)(i - 68) * (FF * 2)) : v4u{0u, 0u, 0u, 0u};
        if (i < 76) return has_mlp ? ldnt(wu_p + (size_t)(i - 72) * (FF * 2)) : v4u{0u, 0u, 0u, 0u};
        if (i < 80) return has_mlp ? ldnt(wd_p + (size_t)(i - 76) * 4 * (DM * 2)) : v4u{0u, 0u, 0u, 0u};
        return ldnt(wo_p + (size_t)(i - 80) * 4 * (DM * 2));
    };

    const v2u xr = *(const GAS v2u*)(p.x + row0);
    v4u ring[R];
#pragma unroll
    for (int i = 0; i < R; ++i) ring[i] = issue(i);
    SB();
    const float xv[4] = {bflo(xr.x), bfhi(xr.x), bflo(xr.y), bfhi(xr.y)};

    {
        float acc[8] = {0.f, 0.f, 0.f, 0.f, 0.f, 0.f, 0.f, 0.f};
#pragma unroll
        for (int t = 0; t < 4; ++t) {
            fma8(acc, ring[t % R], xv[t]);
            ring[t % R] = issue(t + R);
            SB();
        }
        const f32x2 c = reduce_cols(acc);
        if ((lane & 14) == 0)
            *(LAS f32x2*)(L + L_QP + wv * 64 + (8 * (lane & 1) + 4 * (lane >> 5) + 2 * ((lane >> 4) & 1)) * 4) = c;
    }
    WG_BARRIER();

    for (unsigned spins = 0; flags[0] == 0u && spins < (SPIN_CAP << 4); ++spins) __builtin_amdgcn_s_sleep(1);
    asm volatile("" ::: "memory");
    const v4u qf0 = *(const LAS v4u*)(L + L_QF + lane * 16), qf1 = *(const LAS v4u*)(L + L_QF + 1024 + lane * 16);

    float s0[NT], s1[NT];
#pragma unroll
    for (int t = 0; t < NT; ++t) {
        const v4u kf = ring[(4 + t) % R];
        const f32x4 z = {0.f, 0.f, 0.f, 0.f};
        const f32x4 d0 = mfma32(kf, qf0, z), d1 = mfma32(kf, qf1, z);
        ring[(4 + t) % R] = issue(4 + t + R);
        s0[t] = (dpp<0x00>(d0[0]) + dpp<0x55>(d0[1])) + (dpp<0xAA>(d0[2]) + dpp<0xFF>(d0[3]));
        s1[t] = (dpp<0x00>(d1[0]) + dpp<0x55>(d1[1])) + (dpp<0xAA>(d1[2]) + dpp<0xFF>(d1[3]));
        SB();
    }
    constexpr float C2 = 0.088388347648318440f * 1.4426950408889634f;
    float m0 = s0[0], m1 = s1[0];
#pragma unroll
    for (int t = 1; t < NT; ++t) { m0 = fmaxf(m0, s0[t]); m1 = fmaxf(m1, s1[t]); }
    m0 = x16x32_max(m0) * C2; m1 = x16x32_max(m1) * C2;
    float l0 = 0.f, l1 = 0.f;
    const unsigned mk0 = (lane & 3) == 0 ? 0x0000ffffu : (lane & 3) == 1 ? 0xffff0000u : 0u;
    const unsigned mk1 = (lane & 3) == 2 ? 0x0000ffffu : (lane & 3) == 3 ? 0xffff0000u : 0u;
    f32x4 o00 = {0.f, 0.f, 0.f, 0.f}, o01 = o00, o10 = o00, o11 = o00;
#pragma unroll
    for (int t = 0; t < NT; ++t) {
        const float p0 = __builtin_amdgcn_exp2f(fmaf(s0[t], C2, -m0)), p1 = __builtin_amdgcn_exp2f(fmaf(s1[t], C2, -m1));
        l0 += p0; l1 += p1;
        const unsigned pp0 = pk(p0, p0), pp1 = pk(p1, p1);
        const v2u a0 = {pp0 & mk0, pp0 & mk1}, a1 = {pp1 & mk0, pp1 & mk1};
        const v4u vf = ring[(36 + t) % R];
        o00 = mfma16(a0, v2u{vf.x, vf.y}, o00); o01 = mfma16(a0, v2u{vf.z, vf.w}, o01);
        o10 = mfma16(a1, v2u{vf.x, vf.y}, o10); o11 = mfma16(a1, v2u{vf.z, vf.w}, o11);
        if (36 + t + R < 84) ring[(36 + t) % R] = issue(36 + t + R);
        SB();
    }
    l0 = x16x32_sum(l0); l1 = x16x32_sum(l1);
    {
        LAS unsigned char* mb = L + L_MRG + wv * 4096 + (lane >> 4) * 512 + (lane & 15) * 32;
        *(LAS f32x4*)(mb) = o00; *(LAS f32x4*)(mb + 16) = o01;
        *(LAS f32x4*)(mb + 2048) = o10; *(LAS f32x4*)(mb + 2048 + 16) = o11;
        if ((lane & 0x33) == 0) {
            LAS unsigned char* q = L + L_ML + wv * 64 + (lane >> 2) * 8;
            *(LAS f32x2*)(q) = f32x2{m0, l0}; *(LAS f32x2*)(q + 32) = f32x2{m1, l1};
        }
    }
    WG_BARRIER();
    const unsigned tag = flags[2];
    {
        f32x2 ml[NSW];
#pragma unroll
        for (int i = 0; i < NSW; ++i) ml[i] = *(const LAS f32x2*)(L + L_ML + i * 64 + wv * 8);
        float M = ml[0][0];
#pragma unroll
        for (int i = 1; i < NSW; ++i) M = fmaxf(M, ml[i][0]);
        float Ls = 0.f, a0 = 0.f, a1 = 0.f;
#pragma unroll
        for (int i = 0; i < NSW; ++i) {
            const float wt = __builtin_amdgcn_exp2f(ml[i][0] - M);
            const f32x2 o = *(const LAS f32x2*)(L + L_MRG + i * 4096 + wv * 512 + lane * 8);
            Ls = fmaf(wt, ml[i][1], Ls); a0 = fmaf(wt, o[0], a0); a1 = fmaf(wt, o[1], a1);
        }
        const int cons = kvh * 64 + wv * 8;
        st_sc1(rs, WS_X1 + (cons + (lane >> 3)) * X1_STRIDE + sp * 128 + (lane & 7) * 16, v4u{fu(a0), tag, fu(a1), tag});
        if (lane < 8) st_sc1(rs, WS_X1 + (cons + lane) * X1_STRIDE + 8192 + sp * 16, v4u{fu(M), tag, fu(Ls), tag});
    }
    if (has_mlp) {
        float ag[8] = {0.f, 0.f, 0.f, 0.f, 0.f, 0.f, 0.f, 0.f}, au[8] = {0.f, 0.f, 0.f, 0.f, 0.f, 0.f, 0.f, 0.f};
#pragma unroll
        for (int t = 0; t < 4; ++t) { fma8(ag, ring[(68 + t) % R], xv[t]); if (68 + t + R < 84) ring[(68 + t) % R] = issue(68 + t + R); SB(); }
#pragma unroll
        for (int t = 0; t < 4; ++t) { fma8(au, ring[(72 + t) % R], xv[t]); if (72 + t + R < 84) ring[(72 + t) % R] = issue(72 + t + R); SB(); }
        const f32x2 cg = reduce_cols(ag), cu = reduce_cols(au);
        if ((lane & 14) == 0) {
            const int co = (8 * (lane & 1) + 4 * (lane >> 5) + 2 * ((lane >> 4) & 1)) * 4;
            *(LAS f32x2*)(L + L_GU + wv * 128 + co) = cg;
            *(LAS f32x2*)(L + L_GU + wv * 128 + 64 + co) = cu;
        }
        asm volatile("s_waitcnt lgkmcnt(0)" ::: "memory");
        unsigned old = 0;
        if (lane == 0) old = __hip_atomic_fetch_add((LAS unsigned*)(L + L_FLAGS + 4), 1u, __ATOMIC_RELAXED, __HIP_MEMORY_SCOPE_WORKGROUP);
        old = __builtin_amdgcn_readfirstlane(old);
        asm volatile("" ::: "memory");
        if (old == NSW - 1) {
            if (lane < 16) {
                float g = 0.f, u = 0.f;
#pragma unroll
                for (int i = 0; i < NSW; ++i) { g += *(const LAS float*)(L + L_GU + i * 128 + lane * 4); u += *(const LAS float*)(L + L_GU + i * 128 + 64 + lane * 4); }
                g = rbf(g); u = rbf(u);
                const float sg = rbf(g / (1.f + __expf(-g)));
                const float gt = ((const LAS float*)(L + L_SC))[0], rsg = ((const LAS float*)(L + L_SC))[1];
                const float h = rbf(sg * u) * ((1.f - gt) * rsg);
                *(LAS unsigned*)(L + L_HV + lane * 4) = pk(h, h);
            }
        }
    } else {
#pragma unroll
        for (int t = 0; t < 8; ++t) { if (68 + t + R < 84) ring[(68 + t) % R] = issue(68 + t + R); }
    }
    WG_BARRIER();
    {
        f32x4 acc[2] = {{0.f, 0.f, 0.f, 0.f}, {0.f, 0.f, 0.f, 0.f}};
        if (has_mlp) {
#pragma unroll
            for (int t = 0; t < 4; ++t) rows_mfma(acc, ring[(76 + t) % R], *(const LAS unsigned*)(L + L_HV + (4 * t + (lane >> 4)) * 4), mk0, mk1);
        }
#pragma unroll
        for (int t = 0; t < 4; ++t) rows_mfma(acc, ring[(80 + t) % R], *(const LAS unsigned*)(L + L_OV + (4 * t + (lane >> 4)) * 4), mk0, mk1);
        const int g2 = lane >> 4;
        const f32x4 a = (g2 & 2) ? acc[1] : acc[0];
        const float v0 = (g2 & 1) ? a[2] : a[0], v1 = (g2 & 1) ? a[3] : a[1];
        const int cons = 32 * wv + 2 * (lane & 15) + (g2 >> 1);
        st_sc1(rs, WS_X2 + cons * X2_STRIDE + w * 32 + (g2 & 1) * 16, v4u{fu(v0), tag, fu(v1), tag});
    }
}

extern "C" void kernel_launch(void* const* d_in, const int* in_sizes, int n_in, void* d_out, int out_size, void* d_ws, size_t ws_size,
                              hipStream_t stream) {
    Params p;
    p.x = (const unsigned short*)d_in[0];
    p.kc = (const unsigned short*)d_in[1];
    p.vc = (const unsigned short*)d_in[2];
    p.wq = (const unsigned short*)d_in[3];
    p.wo = (const unsigned short*)d_in[4];
    p.wr = (const unsigned short*)d_in[6];
    p.wg = (const unsigned short*)d_in[7];
    p.wu = (const unsigned short*)d_in[8];
    p.wd = (const unsigned short*)d_in[9];
    p.gw = (const unsigned short*)d_in[10];
    p.out = (unsigned short*)d_out;
    p.ws = (unsigned char*)d_ws;
    (void)hipFuncSetAttribute((const void*)fused_decode_kernel, hipFuncAttributeMaxDynamicSharedMemorySize, LDS_BYTES);
    hipLaunchKernelGGL(fused_decode_kernel, dim3(NWG), dim3(NTHR), LDS_BYTES, stream, p);
}
```

```cpp
#include <hip/hip_runtime.h>
#include <hip/hip_bf16.h>
#include <stdint.h>

#define GAS __attribute__((address_space(1)))
#define LAS __attribute__((address_space(3)))

typedef unsigned v4u __attribute__((ext_vector_type(4)));
typedef unsigned v2u __attribute__((ext_vector_type(2)));
typedef float f32x4 __attribute__((ext_vector_type(4)));
typedef float f32x2 __attribute__((ext_vector_type(2)));
typedef short bf16x8 __attribute__((ext_vector_type(8)));
typedef short bf16x4 __attribute__((ext_vector_type(4)));
typedef __bf16 bf2 __attribute__((ext_vector_type(2)));

namespace {

constexpr int NWG = 256, NSW = 8, NTHR = (NSW + 1) * 64;
constexpr int DM = 1024, SK = 65536, DH = 128, FF = 3584, NQ = 4096;
#define RING 20
#define DEPTH 4
constexpr int R = RING;
constexpr int NROUND = 4, TPR = 8;
constexpr int S_GATE = 4, S_UP = 8, S_KV = 12, S_WD = S_KV + NROUND * 2 * TPR, S_WO = S_WD + 4, S_END = S_WO + 4;

constexpr int WS_EPOCH = 0;
constexpr int WS_Q = 4096;
constexpr int WS_Q1 = WS_Q + 2048 * 8;
constexpr int Q1_STRIDE = 32 * 128;
constexpr int WS_ML = WS_Q1 + NWG * Q1_STRIDE;
constexpr int WS_X1 = WS_ML + 32 * 64 * 16;
constexpr int X1_STRIDE = 64 * 128;
constexpr int WS_X2 = WS_X1 + NWG * X1_STRIDE;
constexpr int X2_STRIDE = NWG * 128;
constexpr int WS_H = WS_X2 + 64 * X2_STRIDE;
constexpr int H_STRIDE = 32 * 256;
constexpr int WS_END = WS_H + NWG * H_STRIDE;

constexpr int L_FLAGS = 0;
constexpr int L_HV = 128;
constexpr int L_SC = 192;
constexpr int L_QF = 256;
constexpr int L_ML = L_QF + 2048;
constexpr int L_X1 = L_ML + 512;
constexpr int L_X2 = L_X1 + 64 + 512;
constexpr int L_MRG = L_X2 + 512;
constexpr int L_GU = L_MRG + 8 * 8 * 128 * 4;
constexpr int L_QP = L_GU + 8 * 2 * 448 * 4;
constexpr int L_END = L_QP + 8 * 512 * 4;
constexpr int LDS_BYTES = 96 * 1024;
static_assert(L_END <= LDS_BYTES, "lds");
static_assert(L_MRG % 16 == 0 && L_GU % 16 == 0 && L_QP % 16 == 0 && L_X1 % 16 == 0 && L_X2 % 16 == 0, "align");

constexpr unsigned SPIN_CAP = 1u << 18;
constexpr int AUX_SC1 = 16;

struct Params {
    const unsigned short* x;
    const unsigned short* kc;
    const unsigned short* vc;
    const unsigned short* wq;
    const unsigned short* wo;
    const unsigned short* wr;
    const unsigned short* wg;
    const unsigned short* wu;
    const unsigned short* wd;
    const unsigned short* gw;
    unsigned short* out;
    unsigned char* ws;
};

__device__ __forceinline__ v4u ldnt(const unsigned char* p) { return __builtin_nontemporal_load((const GAS v4u*)p); }
__device__ __forceinline__ float bflo(unsigned u) { return __builtin_bit_cast(float, u << 16); }
__device__ __forceinline__ float bfhi(unsigned u) { return __builtin_bit_cast(float, u & 0xffff0000u); }
__device__ __forceinline__ unsigned pk(float lo, float hi) {
    bf2 r = {(__bf16)lo, (__bf16)hi};
    return __builtin_bit_cast(unsigned, r);
}
__device__ __forceinline__ float rbf(float v) { return bflo(pk(v, v)); }
__device__ __forceinline__ unsigned fu(float f) { return __builtin_bit_cast(unsigned, f); }
__device__ __forceinline__ float uf(unsigned u) { return __builtin_bit_cast(float, u); }
template <int CTRL> __device__ __forceinline__ float dpp(float x) {
    return uf((unsigned)__builtin_amdgcn_mov_dpp((int)fu(x), CTRL, 0xf, 0xf, true));
}
constexpr int XOR1 = 0xB1, XOR2 = 0x4E, XOR7 = 0x141, XOR8 = 0x128, ROR4 = 0x124;
__device__ __forceinline__ float swap32_add(float a, float b) {
    auto s = __builtin_amdgcn_permlane32_swap(fu(a), fu(b), false, false);
    return uf(s[0]) + uf(s[1]);
}
__device__ __forceinline__ float swap16_add(float a, float b) {
    auto s = __builtin_amdgcn_permlane16_swap(fu(a), fu(b), false, false);
    return uf(s[0]) + uf(s[1]);
}
__device__ __forceinline__ float x16x32_max(float x) {
    auto s = __builtin_amdgcn_permlane16_swap(fu(x), fu(x), false, false);
    x = fmaxf(uf(s[0]), uf(s[1]));
    auto t = __builtin_amdgcn_permlane32_swap(fu(x), fu(x), false, false);
    return fmaxf(uf(t[0]), uf(t[1]));
}
__device__ __forceinline__ float x16x32_sum(float x) { x = swap16_add(x, x); return swap32_add(x, x); }
__device__ __forceinline__ float row_sum(float x) {
    x += dpp<XOR1>(x); x += dpp<XOR2>(x); x += dpp<XOR7>(x); x += dpp<XOR8>(x); return x;
}
__device__ __forceinline__ float row_max(float x) {
    x = fmaxf(x, dpp<XOR1>(x)); x = fmaxf(x, dpp<XOR2>(x)); x = fmaxf(x, dpp<XOR7>(x)); x = fmaxf(x, dpp<XOR8>(x)); return x;
}
__device__ __forceinline__ float wave_sum(float x) { return x16x32_sum(row_sum(x)); }
__device__ __forceinline__ float wave_max(float x) { return x16x32_max(row_max(x)); }

__device__ __forceinline__ f32x4 mfma32(v4u a, v4u b, f32x4 c) {
    return __builtin_amdgcn_mfma_f32_16x16x32_bf16(__builtin_bit_cast(bf16x8, a), __builtin_bit_cast(bf16x8, b), c, 0, 0, 0);
}
__device__ __forceinline__ f32x4 mfma16(v2u a, v2u b, f32x4 c) {
    return __builtin_amdgcn_mfma_f32_16x16x16bf16_1k(__builtin_bit_cast(bf16x4, a), __builtin_bit_cast(bf16x4, b), c, 0, 0, 0);
}
#define WG_BARRIER() do { asm volatile("s_waitcnt lgkmcnt(0)" ::: "memory"); __builtin_amdgcn_s_barrier(); asm volatile("" ::: "memory"); } while (0)
#define SB() __builtin_amdgcn_sched_barrier(0)
#define STR2(x) #x
#define STR(x) STR2(x)
#define THROTTLE() asm volatile("s_waitcnt vmcnt(" STR(DEPTHM1) ")" ::: "memory")
#define DEPTHM1 3

__device__ __forceinline__ void fma8(float (&acc)[8], v4u wv, float xv) {
    acc[0] = fmaf(xv, bflo(wv.x), acc[0]); acc[1] = fmaf(xv, bfhi(wv.x), acc[1]);
    acc[2] = fmaf(xv, bflo(wv.y), acc[2]); acc[3] = fmaf(xv, bfhi(wv.y), acc[3]);
    acc[4] = fmaf(xv, bflo(wv.z), acc[4]); acc[5] = fmaf(xv, bfhi(wv.z), acc[5]);
    acc[6] = fmaf(xv, bflo(wv.w), acc[6]); acc[7] = fmaf(xv, bfhi(wv.w), acc[7]);
}
__device__ __forceinline__ f32x2 reduce_cols(const float (&a)[8]) {
    const float b0 = swap32_add(a[0], a[4]), b1 = swap32_add(a[1], a[5]), b2 = swap32_add(a[2], a[6]), b3 = swap32_add(a[3], a[7]);
    float c0 = swap16_add(b0, b2), c1 = swap16_add(b1, b3);
    c0 += dpp<XOR2>(c0); c1 += dpp<XOR2>(c1);
    c0 += dpp<ROR4>(c0); c1 += dpp<ROR4>(c1);
    c0 += dpp<XOR8>(c0); c1 += dpp<XOR8>(c1);
    return f32x2{c0, c1};
}

__device__ __forceinline__ v4u ld_sc1(__amdgpu_buffer_rsrc_t rs, int off) { return __builtin_amdgcn_raw_buffer_load_b128(rs, off, 0, AUX_SC1); }
__device__ __forceinline__ void st_sc1(__amdgpu_buffer_rsrc_t rs, int off, v4u v) { __builtin_amdgcn_raw_buffer_store_b128(v, rs, off, 0, AUX_SC1); }

__device__ __forceinline__ void rows_mfma(f32x4 (&acc)[2], v4u wv, unsigned pp, unsigned m0, unsigned m1) {
    const v2u a = {pp & m0, pp & m1};
    acc[0] = mfma16(a, v2u{wv.x, wv.y}, acc[0]);
    acc[1] = mfma16(a, v2u{wv.z, wv.w}, acc[1]);
}
__device__ __forceinline__ void lds_arrive(LAS unsigned char* L, int word, int lane) {
    asm volatile("s_waitcnt lgkmcnt(0)" ::: "memory");
    if (lane == 0) __hip_atomic_fetch_add((LAS unsigned*)(L + L_FLAGS + 4 * word), 1u, __ATOMIC_RELAXED, __HIP_MEMORY_SCOPE_WORKGROUP);
}
__device__ __forceinline__ void lds_wait_slow(volatile LAS unsigned* flags, int word, unsigned want) {
    for (unsigned spins = 0; flags[word] < want && spins < SPIN_CAP; ++spins) __builtin_amdgcn_s_sleep(16);
    asm volatile("" ::: "memory");
}
__device__ __forceinline__ void lds_wait(volatile LAS unsigned* flags, int word, unsigned want) {
    for (unsigned spins = 0; flags[word] < want && spins < (SPIN_CAP << 4); ++spins) __builtin_amdgcn_s_sleep(1);
    asm volatile("" ::: "memory");
}

#define STAMP(i) do {} while (0)
#define STAMP_DECL() do {} while (0)
#define STAMP_FLUSH() do {} while (0)

}

__global__ void __launch_bounds__(NTHR, 1) fused_decode_kernel(Params p) {
    extern __shared__ __attribute__((aligned(16))) unsigned char smem[];
    LAS unsigned char* L = (LAS unsigned char*)smem;
    volatile LAS unsigned* flags = (volatile LAS unsigned*)(L + L_FLAGS);
    const int tid = threadIdx.x, lane = tid & 63;
    const int wv = __builtin_amdgcn_readfirstlane(tid >> 6);
    const int b = blockIdx.x;
    const int w = (b & 7) * 32 + (b >> 3);
    const int kvh = w >> 6, sp = w & 63;
    const int cb = w & 7, rb = w >> 3;
    const __amdgpu_buffer_rsrc_t rs = __builtin_amdgcn_make_buffer_rsrc((void*)p.ws, 0, WS_END, 0x00020000);
    STAMP_DECL();
    STAMP(0);

    if (wv == NSW) {
        if (lane < 8) flags[lane] = 0u;
        WG_BARRIER();
        const unsigned tag = __builtin_amdgcn_readfirstlane(__builtin_amdgcn_raw_buffer_load_b32(rs, WS_EPOCH, 0, AUX_SC1)) + 1u;
        if (lane == 0) flags[2] = tag;
        bool dead = false;
        STAMP(1);
        lds_wait(flags, 1, NSW);
#pragma unroll
        for (int it = 0; it < 4; ++it) {
            const int c = it * 128 + lane * 2;
            f32x2 s = {0.f, 0.f};
#pragma unroll
            for (int i = 0; i < NSW; ++i) s += *(const LAS f32x2*)(L + L_QP + (i * 512 + c) * 4);
            st_sc1(rs, WS_Q1 + (cb * 32 + (c >> 4)) * Q1_STRIDE + rb * 128 + (c & 15) * 8, v4u{fu(s[0]), tag, fu(s[1]), tag});
        }
        STAMP(2);
        {
            const int xo = WS_Q1 + w * Q1_STRIDE;
            v4u d[4];
            for (unsigned spins = 0;; ++spins) {
                asm volatile("" ::: "memory");
#pragma unroll
                for (int i = 0; i < 4; ++i) d[i] = ld_sc1(rs, xo + i * 1024 + lane * 16);
                bool ok = true;
#pragma unroll
                for (int i = 0; i < 4; ++i) ok = ok && d[i].y == tag && d[i].w == tag;
                if (__all(ok)) break;
                if (spins > SPIN_CAP) { dead = true; break; }
                __builtin_amdgcn_s_sleep(4);
            }
            float s0 = (uf(d[0].x) + uf(d[1].x)) + (uf(d[2].x) + uf(d[3].x)), s1 = (uf(d[0].z) + uf(d[1].z)) + (uf(d[2].z) + uf(d[3].z));
            s0 += dpp<XOR8>(s0); s1 += dpp<XOR8>(s1);
            s0 = x16x32_sum(s0); s1 = x16x32_sum(s1);
            const unsigned pr = pk(s0, s1);
            const unsigned nx = __builtin_amdgcn_mov_dpp((int)pr, XOR1, 0xf, 0xf, true);
            if (lane < 8 && !(lane & 1)) st_sc1(rs, WS_Q + (w * 8 + lane) * 8, v4u{pr, tag, nx, tag});
        }
        STAMP(3);
        lds_wait(flags, 3, NSW);
#pragma unroll
        for (int it = 0; it < 7; ++it) {
            const int idx = it * 64 + lane, j = idx / 14, k = idx - 14 * j;
            const int mat = k >= 7 ? 1 : 0, u = 14 * j + 2 * (k - 7 * mat);
            f32x2 s = {0.f, 0.f};
#pragma unroll
            for (int i = 0; i < NSW; ++i) s += *(const LAS f32x2*)(L + L_GU + ((i * 2 + mat) * 448 + u) * 4);
            st_sc1(rs, WS_H + (cb * 32 + j) * H_STRIDE + rb * 256 + k * 16, v4u{fu(s[0]), tag, fu(s[1]), tag});
        }
        {
            const int qoff = WS_Q + (kvh * 512 + ((lane >> 2) & 3) * 64 + (4 * (lane >> 4) + (lane & 3)) * 4) * 8;
            v4u g0, g1, g2, g3;
            for (unsigned spins = 0;; ++spins) {
                asm volatile("" ::: "memory");
                g0 = ld_sc1(rs, qoff); g1 = ld_sc1(rs, qoff + 16); g2 = ld_sc1(rs, qoff + 2048); g3 = ld_sc1(rs, qoff + 2048 + 16);
                const bool ok = g0.y == tag && g0.w == tag && g1.y == tag && g1.w == tag && g2.y == tag && g2.w == tag && g3.y == tag && g3.w == tag;
                if (__all(ok) || dead) break;
                if (spins > SPIN_CAP) { dead = true; break; }
                __builtin_amdgcn_s_sleep(4);
            }
            *(LAS v4u*)(L + L_QF + lane * 16) = v4u{g0.x, g0.z, g1.x, g1.z};
            *(LAS v4u*)(L + L_QF + 1024 + lane * 16) = v4u{g2.x, g2.z, g3.x, g3.z};
            asm volatile("s_waitcnt lgkmcnt(0)" ::: "memory");
            if (lane == 0) flags[0] = 1u;
        }
        STAMP(4);
        float g_gate, h_scale;
        {
            const GAS v4u* xp = (const GAS v4u*)p.x; const GAS v4u* gp = (const GAS v4u*)p.gw; const GAS v4u* rp = (const GAS v4u*)p.wr;
            float sg = 0.f, sr = 0.f;
#pragma unroll
            for (int i = 0; i < 2; ++i) {
                const v4u xv = xp[lane + 64 * i], gv = gp[lane + 64 * i], rv = rp[lane + 64 * i];
#pragma unroll
                for (int k = 0; k < 4; ++k) {
                    sg += rbf(bflo(xv[k]) * bflo(gv[k])); sg += rbf(bfhi(xv[k]) * bfhi(gv[k]));
                    sr = fmaf(bflo(xv[k]), bflo(rv[k]), sr); sr = fmaf(bfhi(xv[k]), bfhi(rv[k]), sr);
                }
            }
            sg = rbf(wave_sum(sg)); sr = rbf(wave_sum(sr));
            g_gate = rbf(1.f / (1.f + __expf(-sg)));
            const float rsg = rbf(1.f / (1.f + __expf(-sr)));
            h_scale = (1.f - g_gate) * rsg;
            if (lane == 0) *(LAS float*)(L + L_SC) = dead ? __builtin_nanf("") : g_gate;
        }
        lds_wait_slow(flags, 4, 1u);
        {
            const int xo = WS_H + w * H_STRIDE;
            const bool used = (lane & 15) < 14;
            v4u d[8];
            for (unsigned spins = 0;; ++spins) {
                asm volatile("" ::: "memory");
#pragma unroll
                for (int i = 0; i < 8; ++i) d[i] = ld_sc1(rs, xo + i * 1024 + lane * 16);
                bool ok = true;
#pragma unroll
                for (int i = 0; i < 8; ++i) ok = ok && d[i].y == tag && d[i].w == tag;
                if (__all(ok || !used) || dead) break;
                if (spins > SPIN_CAP) { dead = true; break; }
                __builtin_amdgcn_s_sleep(32);
            }
            float s0 = 0.f, s1 = 0.f;
#pragma unroll
            for (int i = 0; i < 8; ++i) { s0 += uf(d[i].x); s1 += uf(d[i].z); }
            s0 = rbf(x16x32_sum(s0)); s1 = rbf(x16x32_sum(s1));
            const float u0 = __shfl(s0, (lane & 15) + 7), u1 = __shfl(s1, (lane & 15) + 7);
            const float a0 = rbf(s0 / (1.f + __expf(-s0))), a1 = rbf(s1 / (1.f + __expf(-s1)));
            float h0 = rbf(a0 * u0) * h_scale, h1 = rbf(a1 * u1) * h_scale;
            if (lane >= 7) { h0 = 0.f; h1 = 0.f; }
            if (dead) { h0 = __builtin_nanf(""); }
            if (lane < 8) *(LAS v2u*)(L + L_HV + lane * 8) = v2u{pk(h0, h0), pk(h1, h1)};
        }
        STAMP(5);
        WG_BARRIER();
        STAMP_FLUSH();
        return;
    }

    const int grow = 32 * rb + 4 * wv;
    const unsigned char* wq_p = (const unsigned char*)p.wq + (size_t)grow * (NQ * 2) + cb * 1024 + lane * 16;
    const size_t kv_base = ((size_t)kvh * SK + (size_t)sp * 1024 + wv * 4) * 256;
    const unsigned char* k_p = (const unsigned char*)p.kc + kv_base + ((lane >> 2) & 3) * 256 + (4 * (lane >> 4) + (lane & 3)) * 16;
    const unsigned char* v_p = (const unsigned char*)p.vc + kv_base + lane * 16;
    const int glane = lane < 56 ? lane : 55;
    const size_t goff = (size_t)grow * (FF * 2) + cb * 896 + glane * 16;
    const unsigned char* wg_p = (const unsigned char*)p.wg + goff;
    const unsigned char* wu_p = (const unsigned char*)p.wu + goff;
    const unsigned char* wd_p = (const unsigned char*)p.wd + (size_t)(w * 14) * (DM * 2) + wv * 256 + (lane & 15) * 16;
    const unsigned char* wo_p = (const unsigned char*)p.wo + (size_t)(w * 16 + (lane >> 4)) * (DM * 2) + wv * 256 + (lane & 15) * 16;

    auto issue = [&](int i) -> v4u {
        if (i < S_GATE) return ldnt(wq_p + (size_t)i * (NQ * 2));
        if (i < S_UP) return ldnt(wg_p + (size_t)(i - S_GATE) * (FF * 2));
        if (i < S_KV) return ldnt(wu_p + (size_t)(i - S_UP) * (FF * 2));
        if (i < S_WD) {
            const int r = (i - S_KV) / (2 * TPR), k = (i - S_KV) % (2 * TPR);
            return k < TPR ? ldnt(k_p + (size_t)(r * TPR + k) * 8192) : ldnt(v_p + (size_t)(r * TPR + k - TPR) * 8192);
        }
        if (i < S_WO) { const int r = 4 * (i - S_WD) + (lane >> 4); return ldnt(wd_p + (size_t)(r < 14 ? r : 13) * (DM * 2)); }
        return ldnt(wo_p + (size_t)(i - S_WO) * 4 * (DM * 2));
    };
    v4u ring[R];
#define REFILL(i) do { if ((i) + R < S_END) { THROTTLE(); ring[(i) % R] = issue((i) + R); } SB(); } while (0)

    const v2u xg = *(const GAS v2u*)(p.x + grow);
#pragma unroll
    for (int i = 0; i < S_KV; ++i) ring[i % R] = issue(i);
    SB();
    WG_BARRIER();
    {
        const float xv[4] = {bflo(xg.x), bfhi(xg.x), bflo(xg.y), bfhi(xg.y)};
        float acc[8] = {0.f, 0.f, 0.f, 0.f, 0.f, 0.f, 0.f, 0.f};
#pragma unroll
        for (int t = 0; t < 4; ++t) fma8(acc, ring[t % R], xv[t]);
        {
            LAS unsigned char* q = L + L_QP + (wv * 512 + lane * 8) * 4;
            *(LAS f32x4*)(q) = f32x4{acc[0], acc[1], acc[2], acc[3]}; *(LAS f32x4*)(q + 16) = f32x4{acc[4], acc[5], acc[6], acc[7]};
        }
        lds_arrive(L, 1, lane);
        STAMP(1);
        float ag[8] = {0.f, 0.f, 0.f, 0.f, 0.f, 0.f, 0.f, 0.f}, au[8] = {0.f, 0.f, 0.f, 0.f, 0.f, 0.f, 0.f, 0.f};
#pragma unroll
        for (int t = 0; t < 4; ++t) { THROTTLE(); ring[(S_KV + t) % R] = issue(S_KV + t); SB(); }
#pragma unroll
        for (int t = 0; t < 4; ++t) { fma8(ag, ring[(S_GATE + t) % R], xv[t]); }
#pragma unroll
        for (int t = 0; t < 4; ++t) { fma8(au, ring[(S_UP + t) % R], xv[t]); }
        if (lane < 56) {
            LAS unsigned char* q = L + L_GU + (wv * 2 * 448 + lane * 8) * 4;
            *(LAS f32x4*)(q) = f32x4{ag[0], ag[1], ag[2], ag[3]}; *(LAS f32x4*)(q + 16) = f32x4{ag[4], ag[5], ag[6], ag[7]};
            *(LAS f32x4*)(q + 1792) = f32x4{au[0], au[1], au[2], au[3]}; *(LAS f32x4*)(q + 1792 + 16) = f32x4{au[4], au[5], au[6], au[7]};
        }
        lds_arrive(L, 3, lane);
    }
#pragma unroll
    for (int i = S_KV + 4; i < S_KV + R; ++i) { THROTTLE(); ring[i % R] = issue(i); SB(); }
    STAMP(2);
    lds_wait(flags, 0, 1u);
    const v4u qf0 = *(const LAS v4u*)(L + L_QF + lane * 16), qf1 = *(const LAS v4u*)(L + L_QF + 1024 + lane * 16);
    STAMP(3);

    constexpr float C2 = 0.088388347648318440f * 1.4426950408889634f;
    const unsigned mk0 = (lane & 3) == 0 ? 0x0000ffffu : (lane & 3) == 1 ? 0xffff0000u : 0u;
    const unsigned mk1 = (lane & 3) == 2 ? 0x0000ffffu : (lane & 3) == 3 ? 0xffff0000u : 0u;
    f32x4 o00 = {0.f, 0.f, 0.f, 0.f}, o01 = o00, o10 = o00, o11 = o00;
    float m0 = 0.f, m1 = 0.f, l0 = 0.f, l1 = 0.f;
#pragma unroll
    for (int r = 0; r < NROUND; ++r) {
        const int base = S_KV + r * 2 * TPR;
        float s0[TPR], s1[TPR];
#pragma unroll
        for (int t = 0; t < TPR; ++t) {
            const v4u kf = ring[(base + t) % R];
            const f32x4 z = {0.f, 0.f, 0.f, 0.f};
            const f32x4 d0 = mfma32(kf, qf0, z), d1 = mfma32(kf, qf1, z);
            REFILL(base + t);
            s0[t] = (dpp<0x00>(d0[0]) + dpp<0x55>(d0[1])) + (dpp<0xAA>(d0[2]) + dpp<0xFF>(d0[3]));
            s1[t] = (dpp<0x00>(d1[0]) + dpp<0x55>(d1[1])) + (dpp<0xAA>(d1[2]) + dpp<0xFF>(d1[3]));
        }
        float t0 = s0[0], t1 = s1[0];
#pragma unroll
        for (int t = 1; t < TPR; ++t) { t0 = fmaxf(t0, s0[t]); t1 = fmaxf(t1, s1[t]); }
        t0 = x16x32_max(t0) * C2; t1 = x16x32_max(t1) * C2;
        if (r == 0) { m0 = t0; m1 = t1; }
        else {
            const float n0 = fmaxf(m0, t0), n1 = fmaxf(m1, t1);
            const float a0 = __builtin_amdgcn_exp2f(m0 - n0), a1 = __builtin_amdgcn_exp2f(m1 - n1);
            m0 = n0; m1 = n1; l0 *= a0; l1 *= a1;
            const float b0 = __shfl(a0, (lane >> 4) * 20), b1 = __shfl(a1, (lane >> 4) * 20);
            o00 *= b0; o01 *= b0; o10 *= b1; o11 *= b1;
        }
#pragma unroll
        for (int t = 0; t < TPR; ++t) {
            const float p0 = __builtin_amdgcn_exp2f(fmaf(s0[t], C2, -m0)), p1 = __builtin_amdgcn_exp2f(fmaf(s1[t], C2, -m1));
            l0 += p0; l1 += p1;
            const unsigned pp0 = pk(p0, p0), pp1 = pk(p1, p1);
            const v2u a0 = {pp0 & mk0, pp0 & mk1}, a1 = {pp1 & mk0, pp1 & mk1};
            const v4u vf = ring[(base + TPR + t) % R];
            o00 = mfma16(a0, v2u{vf.x, vf.y}, o00); o01 = mfma16(a0, v2u{vf.z, vf.w}, o01);
            o10 = mfma16(a1, v2u{vf.x, vf.y}, o10); o11 = mfma16(a1, v2u{vf.z, vf.w}, o11);
            REFILL(base + TPR + t);
        }
        if (r == 1) { STAMP(4); if (wv == 0 && lane == 0) flags[4] = 1u; }
    }
    STAMP(5);
    l0 = x16x32_sum(l0); l1 = x16x32_sum(l1);
    {
        LAS unsigned char* mb = L + L_MRG + wv * 4096 + (lane >> 4) * 512 + (lane & 15) * 32;
        *(LAS f32x4*)(mb) = o00; *(LAS f32x4*)(mb + 16) = o01;
        *(LAS f32x4*)(mb + 2048) = o10; *(LAS f32x4*)(mb + 2048 + 16) = o11;
        if ((lane & 0x33) == 0) {
            LAS unsigned char* q = L + L_ML + wv * 64 + (lane >> 2) * 8;
            *(LAS f32x2*)(q) = f32x2{m0, l0}; *(LAS f32x2*)(q + 32) = f32x2{m1, l1};
        }
    }
    WG_BARRIER();
    STAMP(6);
    const unsigned tag = flags[2];
    {
        f32x2 ml[NSW];
#pragma unroll
        for (int i = 0; i < NSW; ++i) ml[i] = *(const LAS f32x2*)(L + L_ML + i * 64 + wv * 8);
        float M = ml[0][0];
#pragma unroll
        for (int i = 1; i < NSW; ++i) M = fmaxf(M, ml[i][0]);
        float Ls = 0.f, a0 = 0.f, a1 = 0.f;
#pragma unroll
        for (int i = 0; i < NSW; ++i) {
            const float wt = __builtin_amdgcn_exp2f(ml[i][0] - M);
            const f32x2 o = *(const LAS f32x2*)(L + L_MRG + i * 4096 + wv * 512 + lane * 8);
            Ls = fmaf(wt, ml[i][1], Ls); a0 = fmaf(wt, o[0], a0); a1 = fmaf(wt, o[1], a1);
        }
        st_sc1(rs, WS_X1 + (kvh * 64 + wv * 8 + (lane >> 3)) * X1_STRIDE + sp * 128 + (lane & 7) * 16, v4u{fu(a0), tag, fu(a1), tag});
        if (lane == 0) st_sc1(rs, WS_ML + ((kvh * 8 + wv) * 64 + sp) * 16, v4u{fu(M), tag, fu(Ls), tag});
    }
    STAMP(7);
    bool dead = false;
    {
        const int xo = WS_X1 + w * X1_STRIDE + wv * 1024 + lane * 16;
        const int mo = WS_ML + ((w >> 3) * 64 + wv * 8 + (lane >> 3)) * 16;
        v4u dA = ld_sc1(rs, xo), mA = ld_sc1(rs, mo), d, ml;
        __builtin_amdgcn_s_sleep(2);
        v4u dB = ld_sc1(rs, xo), mB = ld_sc1(rs, mo);
        for (unsigned spins = 0;; ++spins) {
            if (__all(dA.y == tag && dA.w == tag && mA.y == tag && mA.w == tag)) { d = dA; ml = mA; break; }
            asm volatile("" ::: "memory");
            dA = ld_sc1(rs, xo); mA = ld_sc1(rs, mo);
            if (__all(dB.y == tag && dB.w == tag && mB.y == tag && mB.w == tag)) { d = dB; ml = mB; break; }
            asm volatile("" ::: "memory");
            dB = ld_sc1(rs, xo); mB = ld_sc1(rs, mo);
            if (spins > SPIN_CAP) { dead = true; d = dA; ml = mA; break; }
        }
        const float m = uf(ml.x);
        float M = fmaxf(m, dpp<XOR8>(m)); M = x16x32_max(M);
        const float wt = __builtin_amdgcn_exp2f(m - M);
        float Ls = wt * uf(ml.z), o0 = wt * uf(d.x), o1 = wt * uf(d.z);
        Ls += dpp<XOR8>(Ls); o0 += dpp<XOR8>(o0); o1 += dpp<XOR8>(o1);
        Ls = x16x32_sum(Ls); o0 = x16x32_sum(o0); o1 = x16x32_sum(o1);
        if (lane < 8) {
            *(LAS float*)(L + L_X1 + 64 + ((2 * lane) * 8 + wv) * 4) = o0;
            *(LAS float*)(L + L_X1 + 64 + ((2 * lane + 1) * 8 + wv) * 4) = o1;
            if (lane == 0) *(LAS f32x2*)(L + L_X1 + wv * 8) = f32x2{M, dead ? __builtin_nanf("") : Ls};
        }
    }
    STAMP(8);
    WG_BARRIER();
    {
        f32x2 ml[NSW];
#pragma unroll
        for (int i = 0; i < NSW; ++i) ml[i] = *(const LAS f32x2*)(L + L_X1 + i * 8);
        float M = ml[0][0];
#pragma unroll
        for (int i = 1; i < NSW; ++i) M = fmaxf(M, ml[i][0]);
        float wt[NSW], Ls = 0.f;
#pragma unroll
        for (int i = 0; i < NSW; ++i) { wt[i] = __builtin_amdgcn_exp2f(ml[i][0] - M); Ls = fmaf(wt[i], ml[i][1], Ls); }
        const float gt = *(const LAS float*)(L + L_SC);
        const float inv = 1.f / Ls;
        f32x4 acc[2] = {{0.f, 0.f, 0.f, 0.f}, {0.f, 0.f, 0.f, 0.f}};
#pragma unroll
        for (int t = 0; t < 4; ++t) rows_mfma(acc, ring[(S_WD + t) % R], *(const LAS unsigned*)(L + L_HV + (4 * t + (lane >> 4)) * 4), mk0, mk1);
#pragma unroll
        for (int t = 0; t < 4; ++t) {
            const LAS unsigned char* q = L + L_X1 + 64 + (4 * t + (lane >> 4)) * 32;
            const f32x4 u = *(const LAS f32x4*)(q), v = *(const LAS f32x4*)(q + 16);
            float o = wt[0] * u[0];
            o = fmaf(wt[1], u[1], o); o = fmaf(wt[2], u[2], o); o = fmaf(wt[3], u[3], o);
            o = fmaf(wt[4], v[0], o); o = fmaf(wt[5], v[1], o); o = fmaf(wt[6], v[2], o); o = fmaf(wt[7], v[3], o);
            o = rbf(o * inv) * gt;
            rows_mfma(acc, ring[(S_WO + t) % R], pk(o, o), mk0, mk1);
        }
        const int g2 = lane >> 4, c = lane & 15;
        const f32x4 a = (g2 & 2) ? acc[1] : acc[0];
        const float v0 = (g2 & 1) ? a[2] : a[0], v1 = (g2 & 1) ? a[3] : a[1];
        const int cons = 8 * wv + (c >> 1);
        st_sc1(rs, WS_X2 + cons * X2_STRIDE + w * 128 + ((c & 1) * 4 + g2) * 16, v4u{fu(v0), tag, fu(v1), tag});
    }
    STAMP(9);
    if ((w & 3) == 0) {
        const int xo = WS_X2 + (w >> 2) * X2_STRIDE + wv * 4096 + lane * 16;
        v4u dA[4], dB[4], d[4];
#pragma unroll
        for (int i = 0; i < 4; ++i) dA[i] = ld_sc1(rs, xo + i * 1024);
        __builtin_amdgcn_s_sleep(2);
#pragma unroll
        for (int i = 0; i < 4; ++i) dB[i] = ld_sc1(rs, xo + i * 1024);
        for (unsigned spins = 0;; ++spins) {
            bool ok = true;
#pragma unroll
            for (int i = 0; i < 4; ++i) ok = ok && dA[i].y == tag && dA[i].w == tag;
            if (__all(ok)) {
#pragma unroll
                for (int i = 0; i < 4; ++i) d[i] = dA[i];
                break;
            }
            asm volatile("" ::: "memory");
#pragma unroll
            for (int i = 0; i < 4; ++i) dA[i] = ld_sc1(rs, xo + i * 1024);
            ok = true;
#pragma unroll
            for (int i = 0; i < 4; ++i) ok = ok && dB[i].y == tag && dB[i].w == tag;
            if (__all(ok)) {
#pragma unroll
                for (int i = 0; i < 4; ++i) d[i] = dB[i];
                break;
            }
            asm volatile("" ::: "memory");
#pragma unroll
            for (int i = 0; i < 4; ++i) dB[i] = ld_sc1(rs, xo + i * 1024);
            if (spins > SPIN_CAP) {
                dead = true;
#pragma unroll
                for (int i = 0; i < 4; ++i) d[i] = dA[i];
                break;
            }
        }
        float s0 = (uf(d[0].x) + uf(d[1].x)) + (uf(d[2].x) + uf(d[3].x)), s1 = (uf(d[0].z) + uf(d[1].z)) + (uf(d[2].z) + uf(d[3].z));
        s0 += dpp<XOR8>(s0); s1 += dpp<XOR8>(s1);
        s0 = x16x32_sum(s0); s1 = x16x32_sum(s1);
        if (dead) s0 = __builtin_nanf("");
        if (lane < 8) *(LAS f32x2*)(L + L_X2 + wv * 64 + lane * 8) = f32x2{s0, s1};
        STAMP(10);
        WG_BARRIER();
        if (wv == 0 && lane < 8) {
            f32x2 s = {0.f, 0.f};
#pragma unroll
            for (int i = 0; i < NSW; ++i) s += *(const LAS f32x2*)(L + L_X2 + i * 64 + lane * 8);
            const float gt = *(const LAS float*)(L + L_SC);
            const int col = 16 * (w >> 2) + 2 * lane;
            const unsigned xv = *(const GAS unsigned*)(p.x + col);
            *(GAS unsigned*)(p.out + col) = pk(fmaf(1.f - gt, bflo(xv), s[0]), fmaf(1.f - gt, bfhi(xv), s[1]));
            if (w == 0 && lane == 0) __builtin_amdgcn_raw_buffer_store_b32(tag, rs, WS_EPOCH, 0, AUX_SC1);
        }
        STAMP(11);
    }
    STAMP_FLUSH();
}

extern "C" void kernel_launch(void* const* d_in, const int* in_sizes, int n_in, void* d_out, int out_size, void* d_ws, size_t ws_size,
                              hipStream_t stream) {
    Params p;
    p.x = (const unsigned short*)d_in[0];
    p.kc = (const unsigned short*)d_in[1];
    p.vc = (const unsigned short*)d_in[2];
    p.wq = (const unsigned short*)d_in[3];
    p.wo = (const unsigned short*)d_in[4];
    p.wr = (const unsigned short*)d_in[6];
    p.wg = (const unsigned short*)d_in[7];
    p.wu = (const unsigned short*)d_in[8];
    p.wd = (const unsigned short*)d_in[9];
    p.gw = (const unsigned short*)d_in[10];
    p.out = (unsigned short*)d_out;
    p.ws = (unsigned char*)d_ws;
    (void)hipFuncSetAttribute((const void*)fused_decode_kernel, hipFuncAttributeMaxDynamicSharedMemorySize, LDS_BYTES);
    hipLaunchKernelGGL(fused_decode_kernel, dim3(NWG), dim3(NTHR), LDS_BYTES, stream, p);
}
```

```cpp
#include <hip/hip_runtime.h>
#include <hip/hip_bf16.h>
#include <stdint.h>

#define GAS __attribute__((address_space(1)))
#define LAS __attribute__((address_space(3)))

typedef unsigned v4u __attribute__((ext_vector_type(4)));
typedef unsigned v2u __attribute__((ext_vector_type(2)));
typedef float f32x4 __attribute__((ext_vector_type(4)));
typedef float f32x2 __attribute__((ext_vector_type(2)));
typedef short bf16x8 __attribute__((ext_vector_type(8)));
typedef short bf16x4 __attribute__((ext_vector_type(4)));
typedef __bf16 bf2 __attribute__((ext_vector_type(2)));

namespace {

constexpr int NWG = 256, NSW = 8, NTHR = (NSW + 1) * 64;
constexpr int DM = 1024, SK = 65536, DH = 128, FF = 3584, NQ = 4096;
#define RING 20
#define DEPTH 4
constexpr int R = RING;
constexpr int NROUND = 4, TPR = 8;
constexpr int S_GATE = 4, S_UP = 8, S_KV = 12, S_WD = S_KV + NROUND * 2 * TPR, S_WO = S_WD + 4, S_END = S_WO + 4;

constexpr int WS_EPOCH = 0;
constexpr int WS_Q = 4096;
constexpr int X1_STRIDE = 64 * 128 + 64 * 16;
constexpr int WS_X1 = WS_Q + 2048 * 8;
constexpr int WS_X2 = WS_X1 + NWG * X1_STRIDE;
constexpr int X2_STRIDE = NWG * 32;
constexpr int WS_H = WS_X2 + NWG * X2_STRIDE;
constexpr int H_STRIDE = 32 * 256;
constexpr int WS_END = WS_H + NWG * H_STRIDE;

constexpr int L_FLAGS = 0;
constexpr int L_SC = 64;
constexpr int L_HV = 128;
constexpr int L_OV = 192;
constexpr int L_QP = 256;
constexpr int L_QF = L_QP + 512;
constexpr int L_ML = L_QF + 2048;
constexpr int L_MRG = L_ML + 512;
constexpr int L_GU = L_MRG + 8 * 8 * 128 * 4;
constexpr int L_END = L_GU + 8 * 2 * 448 * 4;
constexpr int LDS_BYTES = 96 * 1024;
static_assert(L_END <= LDS_BYTES, "lds");

constexpr unsigned SPIN_CAP = 1u << 18;
constexpr int AUX_SC1 = 16;

struct Params {
    const unsigned short* x;
    const unsigned short* kc;
    const unsigned short* vc;
    const unsigned short* wq;
    const unsigned short* wo;
    const unsigned short* wr;
    const unsigned short* wg;
    const unsigned short* wu;
    const unsigned short* wd;
    const unsigned short* gw;
    unsigned short* out;
    unsigned char* ws;
};

__device__ __forceinline__ v4u ldnt(const unsigned char* p) { return __builtin_nontemporal_load((const GAS v4u*)p); }
__device__ __forceinline__ float bflo(unsigned u) { return __builtin_bit_cast(float, u << 16); }
__device__ __forceinline__ float bfhi(unsigned u) { return __builtin_bit_cast(float, u & 0xffff0000u); }
__device__ __forceinline__ unsigned pk(float lo, float hi) {
    bf2 r = {(__bf16)lo, (__bf16)hi};
    return __builtin_bit_cast(unsigned, r);
}
__device__ __forceinline__ float rbf(float v) { return bflo(pk(v, v)); }
__device__ __forceinline__ unsigned fu(float f) { return __builtin_bit_cast(unsigned, f); }
__device__ __forceinline__ float uf(unsigned u) { return __builtin_bit_cast(float, u); }
template <int CTRL> __device__ __forceinline__ float dpp(float x) {
    return uf((unsigned)__builtin_amdgcn_mov_dpp((int)fu(x), CTRL, 0xf, 0xf, true));
}
constexpr int XOR1 = 0xB1, XOR2 = 0x4E, XOR7 = 0x141, XOR8 = 0x128, ROR4 = 0x124;
__device__ __forceinline__ float swap32_add(float a, float b) {
    auto s = __builtin_amdgcn_permlane32_swap(fu(a), fu(b), false, false);
    return uf(s[0]) + uf(s[1]);
}
__device__ __forceinline__ float swap16_add(float a, float b) {
    auto s = __builtin_amdgcn_permlane16_swap(fu(a), fu(b), false, false);
    return uf(s[0]) + uf(s[1]);
}
__device__ __forceinline__ float x16x32_max(float x) {
    auto s = __builtin_amdgcn_permlane16_swap(fu(x), fu(x), false, false);
    x = fmaxf(uf(s[0]), uf(s[1]));
    auto t = __builtin_amdgcn_permlane32_swap(fu(x), fu(x), false, false);
    return fmaxf(uf(t[0]), uf(t[1]));
}
__device__ __forceinline__ float x16x32_sum(float x) { x = swap16_add(x, x); return swap32_add(x, x); }
__device__ __forceinline__ float row_sum(float x) {
    x += dpp<XOR1>(x); x += dpp<XOR2>(x); x += dpp<XOR7>(x); x += dpp<XOR8>(x); return x;
}
__device__ __forceinline__ float row_max(float x) {
    x = fmaxf(x, dpp<XOR1>(x)); x = fmaxf(x, dpp<XOR2>(x)); x = fmaxf(x, dpp<XOR7>(x)); x = fmaxf(x, dpp<XOR8>(x)); return x;
}
__device__ __forceinline__ float wave_sum(float x) { return x16x32_sum(row_sum(x)); }
__device__ __forceinline__ float wave_max(float x) { return x16x32_max(row_max(x)); }

__device__ __forceinline__ f32x4 mfma32(v4u a, v4u b, f32x4 c) {
    return __builtin_amdgcn_mfma_f32_16x16x32_bf16(__builtin_bit_cast(bf16x8, a), __builtin_bit_cast(bf16x8, b), c, 0, 0, 0);
}
__device__ __forceinline__ f32x4 mfma16(v2u a, v2u b, f32x4 c) {
    return __builtin_amdgcn_mfma_f32_16x16x16bf16_1k(__builtin_bit_cast(bf16x4, a), __builtin_bit_cast(bf16x4, b), c, 0, 0, 0);
}
#define WG_BARRIER() do { asm volatile("s_waitcnt lgkmcnt(0)" ::: "memory"); __builtin_amdgcn_s_barrier(); asm volatile("" ::: "memory"); } while (0)
#define SB() __builtin_amdgcn_sched_barrier(0)
#define STR2(x) #x
#define STR(x) STR2(x)
#define THROTTLE() asm volatile("s_waitcnt vmcnt(" STR(DEPTHM1) ")" ::: "memory")
#define DEPTHM1 3

__device__ __forceinline__ void fma8(float (&acc)[8], v4u wv, float xv) {
    acc[0] = fmaf(xv, bflo(wv.x), acc[0]); acc[1] = fmaf(xv, bfhi(wv.x), acc[1]);
    acc[2] = fmaf(xv, bflo(wv.y), acc[2]); acc[3] = fmaf(xv, bfhi(wv.y), acc[3]);
    acc[4] = fmaf(xv, bflo(wv.z), acc[4]); acc[5] = fmaf(xv, bfhi(wv.z), acc[5]);
    acc[6] = fmaf(xv, bflo(wv.w), acc[6]); acc[7] = fmaf(xv, bfhi(wv.w), acc[7]);
}
__device__ __forceinline__ f32x2 reduce_cols(const float (&a)[8]) {
    const float b0 = swap32_add(a[0], a[4]), b1 = swap32_add(a[1], a[5]), b2 = swap32_add(a[2], a[6]), b3 = swap32_add(a[3], a[7]);
    float c0 = swap16_add(b0, b2), c1 = swap16_add(b1, b3);
    c0 += dpp<XOR2>(c0); c1 += dpp<XOR2>(c1);
    c0 += dpp<ROR4>(c0); c1 += dpp<ROR4>(c1);
    c0 += dpp<XOR8>(c0); c1 += dpp<XOR8>(c1);
    return f32x2{c0, c1};
}

__device__ __forceinline__ v4u ld_sc1(__amdgpu_buffer_rsrc_t rs, int off) { return __builtin_amdgcn_raw_buffer_load_b128(rs, off, 0, AUX_SC1); }
__device__ __forceinline__ void st_sc1(__amdgpu_buffer_rsrc_t rs, int off, v4u v) { __builtin_amdgcn_raw_buffer_store_b128(v, rs, off, 0, AUX_SC1); }

__device__ __forceinline__ void rows_mfma(f32x4 (&acc)[2], v4u wv, unsigned pp, unsigned m0, unsigned m1) {
    const v2u a = {pp & m0, pp & m1};
    acc[0] = mfma16(a, v2u{wv.x, wv.y}, acc[0]);
    acc[1] = mfma16(a, v2u{wv.z, wv.w}, acc[1]);
}
__device__ __forceinline__ void lds_arrive(LAS unsigned char* L, int word, int lane) {
    asm volatile("s_waitcnt lgkmcnt(0)" ::: "memory");
    if (lane == 0) __hip_atomic_fetch_add((LAS unsigned*)(L + L_FLAGS + 4 * word), 1u, __ATOMIC_RELAXED, __HIP_MEMORY_SCOPE_WORKGROUP);
}
__device__ __forceinline__ void lds_wait_slow(volatile LAS unsigned* flags, int word, unsigned want) {
    for (unsigned spins = 0; flags[word] < want && spins < SPIN_CAP; ++spins) __builtin_amdgcn_s_sleep(16);
    asm volatile("" ::: "memory");
}
__device__ __forceinline__ void lds_wait(volatile LAS unsigned* flags, int word, unsigned want) {
    for (unsigned spins = 0; flags[word] < want && spins < (SPIN_CAP << 4); ++spins) __builtin_amdgcn_s_sleep(1);
    asm volatile("" ::: "memory");
}

#define STAMP(i) do {} while (0)
#define STAMP_DECL() do {} while (0)
#define STAMP_FLUSH() do {} while (0)

}

__global__ void __launch_bounds__(NTHR, 1) fused_decode_kernel(Params p) {
    extern __shared__ __attribute__((aligned(16))) unsigned char smem[];
    LAS unsigned char* L = (LAS unsigned char*)smem;
    volatile LAS unsigned* flags = (volatile LAS unsigned*)(L + L_FLAGS);
    const int tid = threadIdx.x, lane = tid & 63;
    const int wv = __builtin_amdgcn_readfirstlane(tid >> 6);
    const int b = blockIdx.x;
    const int w = (b & 7) * 32 + (b >> 3);
    const int kvh = w >> 6, sp = w & 63;
    const __amdgpu_buffer_rsrc_t rs = __builtin_amdgcn_make_buffer_rsrc((void*)p.ws, 0, WS_END, 0x00020000);
    STAMP_DECL();
    STAMP(0);

    if (wv == NSW) {
        if (lane < 8) flags[lane] = 0u;
        const unsigned tag = __builtin_amdgcn_readfirstlane(__builtin_amdgcn_raw_buffer_load_b32(rs, WS_EPOCH, 0, AUX_SC1)) + 1u;
        WG_BARRIER();
        if (lane == 0) flags[2] = tag;
        STAMP(1);
        lds_wait(flags, 1, NSW);
        STAMP(2);
        if (lane < 4) {
            f32x4 s = {0.f, 0.f, 0.f, 0.f};
#pragma unroll
            for (int i = 0; i < NSW; ++i) s += *(const LAS f32x4*)(L + L_QP + i * 64 + lane * 16);
            st_sc1(rs, WS_Q + (w * 8 + lane * 2) * 8, v4u{pk(s[0], s[1]), tag, pk(s[2], s[3]), tag});
        }
        bool dead = false;
        STAMP(3);
        lds_wait(flags, 3, NSW);
        {
            const int cb = w & 7, rb = w >> 3;
#pragma unroll
            for (int it = 0; it < 7; ++it) {
                const int idx = it * 64 + lane, j = idx / 14, k = idx - 14 * j;
                const int mat = k >= 7 ? 1 : 0, u = 14 * j + 2 * (k - 7 * mat);
                f32x2 s = {0.f, 0.f};
#pragma unroll
                for (int i = 0; i < NSW; ++i) s += *(const LAS f32x2*)(L + L_GU + ((i * 2 + mat) * 448 + u) * 4);
                st_sc1(rs, WS_H + (cb * 32 + j) * H_STRIDE + rb * 256 + k * 16, v4u{fu(s[0]), tag, fu(s[1]), tag});
            }
        }
        {
            const int qoff = WS_Q + (kvh * 512 + ((lane >> 2) & 3) * 64 + (4 * (lane >> 4) + (lane & 3)) * 4) * 8;
            v4u g0, g1, g2, g3;
            for (unsigned spins = 0;; ++spins) {
                asm volatile("" ::: "memory");
                g0 = ld_sc1(rs, qoff); g1 = ld_sc1(rs, qoff + 16); g2 = ld_sc1(rs, qoff + 2048); g3 = ld_sc1(rs, qoff + 2048 + 16);
                const bool ok = g0.y == tag && g0.w == tag && g1.y == tag && g1.w == tag && g2.y == tag && g2.w == tag && g3.y == tag && g3.w == tag;
                if (__all(ok)) break;
                if (spins > SPIN_CAP) { dead = true; break; }
                __builtin_amdgcn_s_sleep(16);
            }
            *(LAS v4u*)(L + L_QF + lane * 16) = v4u{g0.x, g0.z, g1.x, g1.z};
            *(LAS v4u*)(L + L_QF + 1024 + lane * 16) = v4u{g2.x, g2.z, g3.x, g3.z};
            asm volatile("s_waitcnt lgkmcnt(0)" ::: "memory");
            if (lane == 0) flags[0] = 1u;
        }
        STAMP(4);
        float g_gate, h_scale;
        {
            const GAS v4u* xp = (const GAS v4u*)p.x; const GAS v4u* gp = (const GAS v4u*)p.gw; const GAS v4u* rp = (const GAS v4u*)p.wr;
            float sg = 0.f, sr = 0.f;
#pragma unroll
            for (int i = 0; i < 2; ++i) {
                const v4u xv = xp[lane + 64 * i], gv = gp[lane + 64 * i], rv = rp[lane + 64 * i];
#pragma unroll
                for (int k = 0; k < 4; ++k) {
                    sg += rbf(bflo(xv[k]) * bflo(gv[k])); sg += rbf(bfhi(xv[k]) * bfhi(gv[k]));
                    sr = fmaf(bflo(xv[k]), bflo(rv[k]), sr); sr = fmaf(bfhi(xv[k]), bfhi(rv[k]), sr);
                }
            }
            sg = rbf(wave_sum(sg)); sr = rbf(wave_sum(sr));
            g_gate = rbf(1.f / (1.f + __expf(-sg)));
            const float rsg = rbf(1.f / (1.f + __expf(-sr)));
            h_scale = (1.f - g_gate) * rsg;
        }
        lds_wait_slow(flags, 4, 1u);
        {
            const int xo = WS_H + w * H_STRIDE;
            const bool used = (lane & 15) < 14;
            v4u d[8];
            for (unsigned spins = 0;; ++spins) {
                asm volatile("" ::: "memory");
#pragma unroll
                for (int i = 0; i < 8; ++i) d[i] = ld_sc1(rs, xo + i * 1024 + lane * 16);
                bool ok = true;
#pragma unroll
                for (int i = 0; i < 8; ++i) ok = ok && d[i].y == tag && d[i].w == tag;
                if (__all(ok || !used) || dead) break;
                if (spins > SPIN_CAP) { dead = true; break; }
                __builtin_amdgcn_s_sleep(32);
            }
            float s0 = 0.f, s1 = 0.f;
#pragma unroll
            for (int i = 0; i < 8; ++i) { s0 += uf(d[i].x); s1 += uf(d[i].z); }
            s0 = rbf(x16x32_sum(s0)); s1 = rbf(x16x32_sum(s1));
            const float u0 = __shfl(s0, (lane & 15) + 7), u1 = __shfl(s1, (lane & 15) + 7);
            const float a0 = rbf(s0 / (1.f + __expf(-s0))), a1 = rbf(s1 / (1.f + __expf(-s1)));
            float h0 = rbf(a0 * u0) * h_scale, h1 = rbf(a1 * u1) * h_scale;
            if (lane >= 7) { h0 = 0.f; h1 = 0.f; }
            if (lane < 8) *(LAS v2u*)(L + L_HV + lane * 8) = v2u{pk(h0, h0), pk(h1, h1)};
        }
        STAMP(5);
        WG_BARRIER();
        STAMP(6);
        {
            const int xo = WS_X1 + w * X1_STRIDE;
            v4u d[8], ml;
            unsigned long long got = 0ull;
            unsigned done = 0u;
#pragma unroll
            for (int i = 0; i < 8; ++i) d[i] = v4u{0u, 0u, 0u, 0u};
            for (unsigned spins = 0;; ++spins) {
                asm volatile("" ::: "memory");
                const bool few = __builtin_popcountll(~got) <= 12;
                if (got != ~0ull) ml = ld_sc1(rs, xo + 8192 + lane * 16);
#pragma unroll
                for (int i = 0; i < 8; ++i) {
                    const bool ready = ((got >> (8 * i)) & 0xffull) == 0xffull;
                    if (!((done >> i) & 1u) && (ready || few)) d[i] = ld_sc1(rs, xo + i * 1024 + lane * 16);
                }
                got = __ballot(ml.y == tag && ml.w == tag);
#pragma unroll
                for (int i = 0; i < 8; ++i) if (__all(d[i].y == tag && d[i].w == tag)) done |= 1u << i;
                if ((got == ~0ull && done == 0xffu) || dead) break;
                if (spins > SPIN_CAP) { dead = true; break; }
                __builtin_amdgcn_s_sleep(1);
            }
            STAMP(7);
            const float m = uf(ml.x), l = uf(ml.z);
            const float M = wave_max(m);
            const float wt = __builtin_amdgcn_exp2f(m - M);
            const float Lsum = wave_sum(wt * l);
            float o0 = 0.f, o1 = 0.f;
#pragma unroll
            for (int i = 0; i < 8; ++i) {
                const float wi = __shfl(wt, 8 * i + (lane >> 3));
                o0 = fmaf(wi, uf(d[i].x), o0); o1 = fmaf(wi, uf(d[i].z), o1);
            }
            o0 += dpp<XOR8>(o0); o1 += dpp<XOR8>(o1);
            o0 = x16x32_sum(o0); o1 = x16x32_sum(o1);
            const float inv = 1.f / Lsum;
            o0 = rbf(o0 * inv) * g_gate; o1 = rbf(o1 * inv) * g_gate;
            if (lane < 8) *(LAS v2u*)(L + L_OV + lane * 8) = v2u{pk(o0, o0), pk(o1, o1)};
        }
        WG_BARRIER();
        STAMP(8);
        {
            const int xo = WS_X2 + w * X2_STRIDE;
            const unsigned xv = *(const GAS unsigned*)(p.x + 4 * w + 2 * (lane & 1));
            v4u d[8];
            unsigned done = 0u;
            for (unsigned spins = 0;; ++spins) {
                asm volatile("" ::: "memory");
#pragma unroll
                for (int i = 0; i < 8; ++i) if (!((done >> i) & 1u)) d[i] = ld_sc1(rs, xo + i * 1024 + lane * 16);
#pragma unroll
                for (int i = 0; i < 8; ++i) if (__all(d[i].y == tag && d[i].w == tag)) done |= 1u << i;
                if (done == 0xffu || dead) break;
                if (spins > SPIN_CAP) { dead = true; break; }
                __builtin_amdgcn_s_sleep(1);
            }
            STAMP(9);
            float o0 = 0.f, o1 = 0.f;
#pragma unroll
            for (int i = 0; i < 8; ++i) { o0 += uf(d[i].x); o1 += uf(d[i].z); }
            o0 += dpp<XOR2>(o0); o1 += dpp<XOR2>(o1);
            o0 += dpp<ROR4>(o0); o1 += dpp<ROR4>(o1);
            o0 += dpp<XOR8>(o0); o1 += dpp<XOR8>(o1);
            o0 = x16x32_sum(o0); o1 = x16x32_sum(o1);
            if (lane < 2) {
                const float r0 = fmaf(1.f - g_gate, bflo(xv), o0), r1 = fmaf(1.f - g_gate, bfhi(xv), o1);
                unsigned res = pk(r0, r1);
                if (dead) res = 0x7fc07fc0u;
                *(GAS unsigned*)(p.out + 4 * w + 2 * lane) = res;
            }
            if (w == 0 && lane == 0) __builtin_amdgcn_raw_buffer_store_b32(tag, rs, WS_EPOCH, 0, AUX_SC1);
        }
        STAMP(10);
        STAMP_FLUSH();
        return;
    }

    const int row0 = 128 * wv + 4 * (lane >> 1);
    const unsigned char* wq_p = (const unsigned char*)p.wq + (size_t)row0 * (NQ * 2) + w * 32 + (lane & 1) * 16;
    const size_t kv_base = ((size_t)kvh * SK + (size_t)sp * 1024 + wv * 4) * 256;
    const unsigned char* k_p = (const unsigned char*)p.kc + kv_base + ((lane >> 2) & 3) * 256 + (4 * (lane >> 4) + (lane & 3)) * 16;
    const unsigned char* v_p = (const unsigned char*)p.vc + kv_base + lane * 16;
    const int grow = 32 * (w >> 3) + 4 * wv;
    const int glane = lane < 56 ? lane : 55;
    const size_t goff = (size_t)grow * (FF * 2) + (w & 7) * 896 + glane * 16;
    const unsigned char* wg_p = (const unsigned char*)p.wg + goff;
    const unsigned char* wu_p = (const unsigned char*)p.wu + goff;
    const unsigned char* wd_p = (const unsigned char*)p.wd + (size_t)(w * 14) * (DM * 2) + wv * 256 + (lane & 15) * 16;
    const unsigned char* wo_p = (const unsigned char*)p.wo + (size_t)(w * 16 + (lane >> 4)) * (DM * 2) + wv * 256 + (lane & 15) * 16;

    auto issue = [&](int i) -> v4u {
        if (i < S_GATE) return ldnt(wq_p + (size_t)i * (NQ * 2));
        if (i < S_UP) return ldnt(wg_p + (size_t)(i - S_GATE) * (FF * 2));
        if (i < S_KV) return ldnt(wu_p + (size_t)(i - S_UP) * (FF * 2));
        if (i < S_WD) {
            const int r = (i - S_KV) / (2 * TPR), k = (i - S_KV) % (2 * TPR);
            return k < TPR ? ldnt(k_p + (size_t)(r * TPR + k) * 8192) : ldnt(v_p + (size_t)(r * TPR + k - TPR) * 8192);
        }
        if (i < S_WO) { const int r = 4 * (i - S_WD) + (lane >> 4); return ldnt(wd_p + (size_t)(r < 14 ? r : 13) * (DM * 2)); }
        return ldnt(wo_p + (size_t)(i - S_WO) * 4 * (DM * 2));
    };
    v4u ring[R];
#define REFILL(i) do { if ((i) + R < S_END) { THROTTLE(); ring[(i) % R] = issue((i) + R); } SB(); } while (0)

    const v2u xr = *(const GAS v2u*)(p.x + row0);
    const v2u xg = *(const GAS v2u*)(p.x + grow);
#pragma unroll
    for (int i = 0; i < S_KV; ++i) ring[i % R] = issue(i);
    SB();
    WG_BARRIER();
    {
        const float xv[4] = {bflo(xr.x), bfhi(xr.x), bflo(xr.y), bfhi(xr.y)};
        float acc[8] = {0.f, 0.f, 0.f, 0.f, 0.f, 0.f, 0.f, 0.f};
#pragma unroll
        for (int t = 0; t < 4; ++t) fma8(acc, ring[t % R], xv[t]);
        const f32x2 c = reduce_cols(acc);
        if ((lane & 14) == 0)
            *(LAS f32x2*)(L + L_QP + wv * 64 + (8 * (lane & 1) + 4 * (lane >> 5) + 2 * ((lane >> 4) & 1)) * 4) = c;
        lds_arrive(L, 1, lane);
    }
    STAMP(1);
    {
        const float xv[4] = {bflo(xg.x), bfhi(xg.x), bflo(xg.y), bfhi(xg.y)};
        float ag[8] = {0.f, 0.f, 0.f, 0.f, 0.f, 0.f, 0.f, 0.f}, au[8] = {0.f, 0.f, 0.f, 0.f, 0.f, 0.f, 0.f, 0.f};
#pragma unroll
        for (int t = 0; t < 4; ++t) { THROTTLE(); ring[(S_KV + t) % R] = issue(S_KV + t); SB(); }
#pragma unroll
        for (int t = 0; t < 4; ++t) { fma8(ag, ring[(S_GATE + t) % R], xv[t]); }
#pragma unroll
        for (int t = 0; t < 4; ++t) { fma8(au, ring[(S_UP + t) % R], xv[t]); }
        if (lane < 56) {
            LAS unsigned char* q = L + L_GU + (wv * 2 * 448 + lane * 8) * 4;
            *(LAS f32x4*)(q) = f32x4{ag[0], ag[1], ag[2], ag[3]}; *(LAS f32x4*)(q + 16) = f32x4{ag[4], ag[5], ag[6], ag[7]};
            *(LAS f32x4*)(q + 1792) = f32x4{au[0], au[1], au[2], au[3]}; *(LAS f32x4*)(q + 1792 + 16) = f32x4{au[4], au[5], au[6], au[7]};
        }
        lds_arrive(L, 3, lane);
    }
#pragma unroll
    for (int i = S_KV + 4; i < S_KV + R; ++i) { THROTTLE(); ring[i % R] = issue(i); SB(); }
    STAMP(2);
    lds_wait(flags, 0, 1u);
    const v4u qf0 = *(const LAS v4u*)(L + L_QF + lane * 16), qf1 = *(const LAS v4u*)(L + L_QF + 1024 + lane * 16);
    STAMP(3);

    constexpr float C2 = 0.088388347648318440f * 1.4426950408889634f;
    const unsigned mk0 = (lane & 3) == 0 ? 0x0000ffffu : (lane & 3) == 1 ? 0xffff0000u : 0u;
    const unsigned mk1 = (lane & 3) == 2 ? 0x0000ffffu : (lane & 3) == 3 ? 0xffff0000u : 0u;
    f32x4 o00 = {0.f, 0.f, 0.f, 0.f}, o01 = o00, o10 = o00, o11 = o00;
    float m0 = 0.f, m1 = 0.f, l0 = 0.f, l1 = 0.f;
#pragma unroll
    for (int r = 0; r < NROUND; ++r) {
        const int base = S_KV + r * 2 * TPR;
        float s0[TPR], s1[TPR];
#pragma unroll
        for (int t = 0; t < TPR; ++t) {
            const v4u kf = ring[(base + t) % R];
            const f32x4 z = {0.f, 0.f, 0.f, 0.f};
            const f32x4 d0 = mfma32(kf, qf0, z), d1 = mfma32(kf, qf1, z);
            REFILL(base + t);
            s0[t] = (dpp<0x00>(d0[0]) + dpp<0x55>(d0[1])) + (dpp<0xAA>(d0[2]) + dpp<0xFF>(d0[3]));
            s1[t] = (dpp<0x00>(d1[0]) + dpp<0x55>(d1[1])) + (dpp<0xAA>(d1[2]) + dpp<0xFF>(d1[3]));
        }
        float t0 = s0[0], t1 = s1[0];
#pragma unroll
        for (int t = 1; t < TPR; ++t) { t0 = fmaxf(t0, s0[t]); t1 = fmaxf(t1, s1[t]); }
        t0 = x16x32_max(t0) * C2; t1 = x16x32_max(t1) * C2;
        if (r == 0) { m0 = t0; m1 = t1; }
        else {
            const float n0 = fmaxf(m0, t0), n1 = fmaxf(m1, t1);
            const float a0 = __builtin_amdgcn_exp2f(m0 - n0), a1 = __builtin_amdgcn_exp2f(m1 - n1);
            m0 = n0; m1 = n1; l0 *= a0; l1 *= a1;
            const float b0 = __shfl(a0, (lane >> 4) * 20), b1 = __shfl(a1, (lane >> 4) * 20);
            o00 *= b0; o01 *= b0; o10 *= b1; o11 *= b1;
        }
#pragma unroll
        for (int t = 0; t < TPR; ++t) {
            const float p0 = __builtin_amdgcn_exp2f(fmaf(s0[t], C2, -m0)), p1 = __builtin_amdgcn_exp2f(fmaf(s1[t], C2, -m1));
            l0 += p0; l1 += p1;
            const unsigned pp0 = pk(p0, p0), pp1 = pk(p1, p1);
            const v2u a0 = {pp0 & mk0, pp0 & mk1}, a1 = {pp1 & mk0, pp1 & mk1};
            const v4u vf = ring[(base + TPR + t) % R];
            o00 = mfma16(a0, v2u{vf.x, vf.y}, o00); o01 = mfma16(a0, v2u{vf.z, vf.w}, o01);
            o10 = mfma16(a1, v2u{vf.x, vf.y}, o10); o11 = mfma16(a1, v2u{vf.z, vf.w}, o11);
            REFILL(base + TPR + t);
        }
        if (r == 1) { STAMP(4); if (wv == 0 && lane == 0) flags[4] = 1u; }
    }
    STAMP(5);
    l0 = x16x32_sum(l0); l1 = x16x32_sum(l1);
    {
        LAS unsigned char* mb = L + L_MRG + wv * 4096 + (lane >> 4) * 512 + (lane & 15) * 32;
        *(LAS f32x4*)(mb) = o00; *(LAS f32x4*)(mb + 16) = o01;
        *(LAS f32x4*)(mb + 2048) = o10; *(LAS f32x4*)(mb + 2048 + 16) = o11;
        if ((lane & 0x33) == 0) {
            LAS unsigned char* q = L + L_ML + wv * 64 + (lane >> 2) * 8;
            *(LAS f32x2*)(q) = f32x2{m0, l0}; *(LAS f32x2*)(q + 32) = f32x2{m1, l1};
        }
    }
    WG_BARRIER();
    STAMP(6);
    const unsigned tag = flags[2];
    {
        f32x2 ml[NSW];
#pragma unroll
        for (int i = 0; i < NSW; ++i) ml[i] = *(const LAS f32x2*)(L + L_ML + i * 64 + wv * 8);
        float M = ml[0][0];
#pragma unroll
        for (int i = 1; i < NSW; ++i) M = fmaxf(M, ml[i][0]);
        float Ls = 0.f, a0 = 0.f, a1 = 0.f;
#pragma unroll
        for (int i = 0; i < NSW; ++i) {
            const float wt = __builtin_amdgcn_exp2f(ml[i][0] - M);
            const f32x2 o = *(const LAS f32x2*)(L + L_MRG + i * 4096 + wv * 512 + lane * 8);
            Ls = fmaf(wt, ml[i][1], Ls); a0 = fmaf(wt, o[0], a0); a1 = fmaf(wt, o[1], a1);
        }
        const int cons = kvh * 64 + wv * 8;
        st_sc1(rs, WS_X1 + (cons + (lane >> 3)) * X1_STRIDE + sp * 128 + (lane & 7) * 16, v4u{fu(a0), tag, fu(a1), tag});
        if (lane < 8) st_sc1(rs, WS_X1 + (cons + lane) * X1_STRIDE + 8192 + sp * 16, v4u{fu(M), tag, fu(Ls), tag});
    }
    STAMP(7);
    WG_BARRIER();
    STAMP(8);
    {
        f32x4 acc[2] = {{0.f, 0.f, 0.f, 0.f}, {0.f, 0.f, 0.f, 0.f}};
#pragma unroll
        for (int t = 0; t < 4; ++t) rows_mfma(acc, ring[(S_WD + t) % R], *(const LAS unsigned*)(L + L_HV + (4 * t + (lane >> 4)) * 4), mk0, mk1);
#pragma unroll
        for (int t = 0; t < 4; ++t) rows_mfma(acc, ring[(S_WO + t) % R], *(const LAS unsigned*)(L + L_OV + (4 * t + (lane >> 4)) * 4), mk0, mk1);
        const int g2 = lane >> 4;
        const f32x4 a = (g2 & 2) ? acc[1] : acc[0];
        const float v0 = (g2 & 1) ? a[2] : a[0], v1 = (g2 & 1) ? a[3] : a[1];
        const int cons = 32 * wv + 2 * (lane & 15) + (g2 >> 1);
        st_sc1(rs, WS_X2 + cons * X2_STRIDE + w * 32 + (g2 & 1) * 16, v4u{fu(v0), tag, fu(v1), tag});
    }
    STAMP(9);
    STAMP_FLUSH();
}

extern "C" void kernel_launch(void* const* d_in, const int* in_sizes, int n_in, void* d_out, int out_size, void* d_ws, size_t ws_size,
                              hipStream_t stream) {
    Params p;
    p.x = (const unsigned short*)d_in[0];
    p.kc = (const unsigned short*)d_in[1];
    p.vc = (const unsigned short*)d_in[2];
    p.wq = (const unsigned short*)d_in[3];
    p.wo = (const unsigned short*)d_in[4];
    p.wr = (const unsigned short*)d_in[6];
    p.wg = (const unsigned short*)d_in[7];
    p.wu = (const unsigned short*)d_in[8];
    p.wd = (const unsigned short*)d_in[9];
    p.gw = (const unsigned short*)d_in[10];
    p.out = (unsigned short*)d_out;
    p.ws = (unsigned char*)d_ws;
    (void)hipFuncSetAttribute((const void*)fused_decode_kernel, hipFuncAttributeMaxDynamicSharedMemorySize, LDS_BYTES);
    hipLaunchKernelGGL(fused_decode_kernel, dim3(NWG), dim3(NTHR), LDS_BYTES, stream, p);
}
```

```cpp
#include <hip/hip_runtime.h>
#include <hip/hip_bf16.h>
#include <stdint.h>

#define GAS __attribute__((address_space(1)))
#define LAS __attribute__((address_space(3)))

typedef unsigned v4u __attribute__((ext_vector_type(4)));
typedef unsigned v2u __attribute__((ext_vector_type(2)));
typedef float f32x4 __attribute__((ext_vector_type(4)));
typedef float f32x2 __attribute__((ext_vector_type(2)));
typedef short bf16x8 __attribute__((ext_vector_type(8)));
typedef short bf16x4 __attribute__((ext_vector_type(4)));
typedef __bf16 bf2 __attribute__((ext_vector_type(2)));

namespace {

constexpr int NWG = 256, NSW = 8, NTHR = (NSW + 1) * 64;
constexpr int DM = 1024, SK = 65536, DH = 128, FF = 3584, NQ = 4096;
#define EARLY 2
#define TAILD 4
#define TAILDM1 3
#define PRIO 0
#define DEPTH 4
#define DTW 28
#define PROG1 4
#define PROG2 13
constexpr int TPR = 4, NROUND = 8;
constexpr int E_END = 64;
constexpr int DT = DTW;
constexpr int jmax(int e) {
    const int a = DT - 1 + (e + 2) / 2, b = e + DEPTH + 1;
    const int m = a > b ? a : b;
    return m < E_END - 1 ? m : E_END - 1;
}

constexpr int WS_EPOCH = 0;
constexpr int WS_Q = 4096;
constexpr int WS_Q1 = WS_Q + 2048 * 8;
constexpr int Q1_STRIDE = 32 * 128;
constexpr int WS_ML = WS_Q1 + NWG * Q1_STRIDE;
constexpr int WS_X1 = WS_ML + 32 * 64 * 16;
constexpr int X1_STRIDE = 64 * 128;
constexpr int WS_X2 = WS_X1 + NWG * X1_STRIDE;
constexpr int X2_STRIDE = NWG * 128;
constexpr int WS_H = WS_X2 + 64 * X2_STRIDE;
constexpr int H_STRIDE = 32 * 256;
constexpr int WS_END = WS_H + NWG * H_STRIDE;

constexpr int L_FLAGS = 0;
constexpr int L_HV = 128;
constexpr int L_SC = 192;
constexpr int L_QF = 256;
constexpr int L_ML = L_QF + 2048;
constexpr int L_X1 = L_ML + 512;
constexpr int L_X2 = L_X1 + 64 + 512;
constexpr int L_GU = L_X2 + 512;
constexpr int L_QP = L_GU + 8 * 2 * 448 * 4;
constexpr int L_MRG = L_QP + 8 * 512 * 4;
constexpr int L_END = L_MRG + 8 * 8 * 128 * 4;
constexpr int LDS_BYTES = 88 * 1024;
static_assert(L_END <= LDS_BYTES, "lds");
static_assert(LDS_BYTES <= 160 * 1024 && LDS_BYTES > 80 * 1024, "lds");
static_assert(L_MRG % 16 == 0 && L_GU % 16 == 0 && L_QP % 16 == 0, "align");

constexpr unsigned SPIN_CAP = 1u << 18;
constexpr int AUX_SC1 = 16;

struct Params {
    const unsigned short* x;
    const unsigned short* kc;
    const unsigned short* vc;
    const unsigned short* wq;
    const unsigned short* wo;
    const unsigned short* wr;
    const unsigned short* wg;
    const unsigned short* wu;
    const unsigned short* wd;
    const unsigned short* gw;
    unsigned short* out;
    unsigned char* ws;
};

__device__ __forceinline__ v4u ldnt(const unsigned char* p) { return __builtin_nontemporal_load((const GAS v4u*)p); }
__device__ __forceinline__ float bflo(unsigned u) { return __builtin_bit_cast(float, u << 16); }
__device__ __forceinline__ float bfhi(unsigned u) { return __builtin_bit_cast(float, u & 0xffff0000u); }
__device__ __forceinline__ unsigned pk(float lo, float hi) {
    bf2 r = {(__bf16)lo, (__bf16)hi};
    return __builtin_bit_cast(unsigned, r);
}
__device__ __forceinline__ float rbf(float v) { return bflo(pk(v, v)); }
__device__ __forceinline__ unsigned fu(float f) { return __builtin_bit_cast(unsigned, f); }
__device__ __forceinline__ float uf(unsigned u) { return __builtin_bit_cast(float, u); }
template <int CTRL> __device__ __forceinline__ float dpp(float x) {
    return uf((unsigned)__builtin_amdgcn_mov_dpp((int)fu(x), CTRL, 0xf, 0xf, true));
}
constexpr int XOR1 = 0xB1, XOR2 = 0x4E, XOR7 = 0x141, XOR8 = 0x128, ROR4 = 0x124;
__device__ __forceinline__ float swap32_add(float a, float b) {
    auto s = __builtin_amdgcn_permlane32_swap(fu(a), fu(b), false, false);
    return uf(s[0]) + uf(s[1]);
}
__device__ __forceinline__ float swap16_add(float a, float b) {
    auto s = __builtin_amdgcn_permlane16_swap(fu(a), fu(b), false, false);
    return uf(s[0]) + uf(s[1]);
}
__device__ __forceinline__ float x16x32_max(float x) {
    auto s = __builtin_amdgcn_permlane16_swap(fu(x), fu(x), false, false);
    x = fmaxf(uf(s[0]), uf(s[1]));
    auto t = __builtin_amdgcn_permlane32_swap(fu(x), fu(x), false, false);
    return fmaxf(uf(t[0]), uf(t[1]));
}
__device__ __forceinline__ float x16x32_sum(float x) { x = swap16_add(x, x); return swap32_add(x, x); }
__device__ __forceinline__ float row_sum(float x) {
    x += dpp<XOR1>(x); x += dpp<XOR2>(x); x += dpp<XOR7>(x); x += dpp<XOR8>(x); return x;
}
__device__ __forceinline__ float row_max(float x) {
    x = fmaxf(x, dpp<XOR1>(x)); x = fmaxf(x, dpp<XOR2>(x)); x = fmaxf(x, dpp<XOR7>(x)); x = fmaxf(x, dpp<XOR8>(x)); return x;
}
__device__ __forceinline__ float wave_sum(float x) { return x16x32_sum(row_sum(x)); }
__device__ __forceinline__ float wave_max(float x) { return x16x32_max(row_max(x)); }

__device__ __forceinline__ f32x4 mfma32(v4u a, v4u b, f32x4 c) {
    return __builtin_amdgcn_mfma_f32_16x16x32_bf16(__builtin_bit_cast(bf16x8, a), __builtin_bit_cast(bf16x8, b), c, 0, 0, 0);
}
__device__ __forceinline__ f32x4 mfma16(v2u a, v2u b, f32x4 c) {
    return __builtin_amdgcn_mfma_f32_16x16x16bf16_1k(__builtin_bit_cast(bf16x4, a), __builtin_bit_cast(bf16x4, b), c, 0, 0, 0);
}
#define WG_BARRIER() do { asm volatile("s_waitcnt lgkmcnt(0)" ::: "memory"); __builtin_amdgcn_s_barrier(); asm volatile("" ::: "memory"); } while (0)
#define SB() __builtin_amdgcn_sched_barrier(0)
#define SBMASK 0x10F
#define SBM() __builtin_amdgcn_sched_barrier(SBMASK)
#define STR2(x) #x
#define STR(x) STR2(x)
#define THROTTLE() asm volatile("s_waitcnt vmcnt(" STR(DEPTHM1) ")" ::: "memory")
#define DEPTHM1 3

__device__ __forceinline__ void fma8(float (&acc)[8], v4u wv, float xv) {
    acc[0] = fmaf(xv, bflo(wv.x), acc[0]); acc[1] = fmaf(xv, bfhi(wv.x), acc[1]);
    acc[2] = fmaf(xv, bflo(wv.y), acc[2]); acc[3] = fmaf(xv, bfhi(wv.y), acc[3]);
    acc[4] = fmaf(xv, bflo(wv.z), acc[4]); acc[5] = fmaf(xv, bfhi(wv.z), acc[5]);
    acc[6] = fmaf(xv, bflo(wv.w), acc[6]); acc[7] = fmaf(xv, bfhi(wv.w), acc[7]);
}
__device__ __forceinline__ f32x2 reduce_cols(const float (&a)[8]) {
    const float b0 = swap32_add(a[0], a[4]), b1 = swap32_add(a[1], a[5]), b2 = swap32_add(a[2], a[6]), b3 = swap32_add(a[3], a[7]);
    float c0 = swap16_add(b0, b2), c1 = swap16_add(b1, b3);
    c0 += dpp<XOR2>(c0); c1 += dpp<XOR2>(c1);
    c0 += dpp<ROR4>(c0); c1 += dpp<ROR4>(c1);
    c0 += dpp<XOR8>(c0); c1 += dpp<XOR8>(c1);
    return f32x2{c0, c1};
}

__device__ __forceinline__ v4u ld_sc1(__amdgpu_buffer_rsrc_t rs, int off) { return __builtin_amdgcn_raw_buffer_load_b128(rs, off, 0, AUX_SC1); }
__device__ __forceinline__ void st_sc1(__amdgpu_buffer_rsrc_t rs, int off, v4u v) { __builtin_amdgcn_raw_buffer_store_b128(v, rs, off, 0, AUX_SC1); }

__device__ __forceinline__ void rows_mfma(f32x4 (&acc)[2], v4u wv, unsigned pp, unsigned m0, unsigned m1) {
    const v2u a = {pp & m0, pp & m1};
    acc[0] = mfma16(a, v2u{wv.x, wv.y}, acc[0]);
    acc[1] = mfma16(a, v2u{wv.z, wv.w}, acc[1]);
}
__device__ __forceinline__ unsigned lds_arrive(LAS unsigned char* L, int word, int lane) {
    asm volatile("s_waitcnt lgkmcnt(0)" ::: "memory");
    unsigned old = 0;
    if (lane == 0) old = __hip_atomic_fetch_add((LAS unsigned*)(L + L_FLAGS + 4 * word), 1u, __ATOMIC_RELAXED, __HIP_MEMORY_SCOPE_WORKGROUP);
    old = __builtin_amdgcn_readfirstlane(old);
    asm volatile("" ::: "memory");
    return old;
}
__device__ __forceinline__ void lds_wait_slow(volatile LAS unsigned* flags, int word, unsigned want) {
    for (unsigned spins = 0; flags[word] < want && spins < SPIN_CAP; ++spins) __builtin_amdgcn_s_sleep(8);
    asm volatile("" ::: "memory");
}
__device__ __forceinline__ void lds_wait(volatile LAS unsigned* flags, int word, unsigned want) {
    for (unsigned spins = 0; flags[word] < want && spins < (SPIN_CAP << 4); ++spins) __builtin_amdgcn_s_sleep(1);
    asm volatile("" ::: "memory");
}
__device__ __forceinline__ float qred(f32x4 d) {
    float t = dpp<0x00>(d[0]);
    t = dpp<0x55>(d[1]) + t;
    asm volatile("" : "+v"(t));
    t = dpp<0xAA>(d[2]) + t;
    t = dpp<0xFF>(d[3]) + t;
    return t;
}
template <int T> __device__ __forceinline__ unsigned qbc(unsigned x) {
    return (unsigned)__builtin_amdgcn_mov_dpp((int)x, T * 0x55, 0xf, 0xf, true);
}

#define STAMP(i) do {} while (0)
#define STAMP_DECL() do {} while (0)
#define STAMP_FLUSH() do {} while (0)

}

__global__ void __launch_bounds__(NTHR, 1) fused_decode_kernel(Params p) {
    extern __shared__ __attribute__((aligned(16))) unsigned char smem[];
    LAS unsigned char* L = (LAS unsigned char*)smem;
    volatile LAS unsigned* flags = (volatile LAS unsigned*)(L + L_FLAGS);
    const int tid = threadIdx.x, lane = tid & 63;
    const int wv = __builtin_amdgcn_readfirstlane(tid >> 6);
    const int b = blockIdx.x;
    const int w = (b & 7) * 32 + (b >> 3);
    const int kvh = w >> 6, sp = w & 63;
    const int cb = w & 7, rb = w >> 3;
    const __amdgpu_buffer_rsrc_t rs = __builtin_amdgcn_make_buffer_rsrc((void*)p.ws, 0, WS_END, 0x00020000);
    STAMP_DECL();
    STAMP(0);

    if (wv == NSW) {
        const unsigned ep = __builtin_amdgcn_raw_buffer_load_b32(rs, WS_EPOCH, 0, AUX_SC1);
        if (lane < 8) flags[lane] = 0u;
        WG_BARRIER();
        const unsigned tag = __builtin_amdgcn_readfirstlane(ep) + 1u;
        if (lane == 0) flags[3] = tag;
        bool dead = false;
        STAMP(1);
        lds_wait(flags, 1, NSW);
#pragma unroll
        for (int it = 0; it < 4; ++it) {
            const int c = it * 128 + lane * 2;
            f32x2 s = {0.f, 0.f};
#pragma unroll
            for (int i = 0; i < NSW; ++i) s += *(const LAS f32x2*)(L + L_QP + (i * 512 + c) * 4);
            st_sc1(rs, WS_Q1 + (cb * 32 + (c >> 4)) * Q1_STRIDE + rb * 128 + (c & 15) * 8, v4u{fu(s[0]), tag, fu(s[1]), tag});
        }
        STAMP(2);
        lds_wait_slow(flags, 4, 1u);
        {
            const int xo = WS_Q1 + w * Q1_STRIDE;
            v4u d[4];
            for (unsigned spins = 0;; ++spins) {
                asm volatile("" ::: "memory");
#pragma unroll
                for (int i = 0; i < 4; ++i) d[i] = ld_sc1(rs, xo + i * 1024 + lane * 16);
                bool ok = true;
#pragma unroll
                for (int i = 0; i < 4; ++i) ok = ok && d[i].y == tag && d[i].w == tag;
                if (__all(ok)) break;
                if (spins > SPIN_CAP) { dead = true; break; }
                __builtin_amdgcn_s_sleep(8);
            }
            float s0 = (uf(d[0].x) + uf(d[1].x)) + (uf(d[2].x) + uf(d[3].x)), s1 = (uf(d[0].z) + uf(d[1].z)) + (uf(d[2].z) + uf(d[3].z));
            s0 += dpp<XOR8>(s0); s1 += dpp<XOR8>(s1);
            s0 = x16x32_sum(s0); s1 = x16x32_sum(s1);
            const unsigned pr = pk(s0, s1);
            const unsigned nx = __builtin_amdgcn_mov_dpp((int)pr, XOR1, 0xf, 0xf, true);
            if (lane < 8 && !(lane & 1) && !dead) st_sc1(rs, WS_Q + (w * 8 + lane) * 8, v4u{pr, tag, nx, tag});
        }
        STAMP(3);
        lds_wait(flags, 2, NSW);
#pragma unroll
        for (int it = 0; it < 7; ++it) {
            const int idx = it * 64 + lane, j = idx / 14, k = idx - 14 * j;
            const int mat = k >= 7 ? 1 : 0, u = 14 * j + 2 * (k - 7 * mat);
            f32x2 s = {0.f, 0.f};
#pragma unroll
            for (int i = 0; i < NSW; ++i) s += *(const LAS f32x2*)(L + L_GU + ((i * 2 + mat) * 448 + u) * 4);
            st_sc1(rs, WS_H + (cb * 32 + j) * H_STRIDE + rb * 256 + k * 16, v4u{fu(s[0]), tag, fu(s[1]), tag});
        }
        lds_wait_slow(flags, 5, 1u);
        {
            const int qoff = WS_Q + (kvh * 512 + ((lane >> 2) & 3) * 64 + (4 * (lane >> 4) + (lane & 3)) * 4) * 8;
            v4u g0, g1, g2, g3;
            for (unsigned spins = 0;; ++spins) {
                asm volatile("" ::: "memory");
                g0 = ld_sc1(rs, qoff); g1 = ld_sc1(rs, qoff + 16); g2 = ld_sc1(rs, qoff + 2048); g3 = ld_sc1(rs, qoff + 2048 + 16);
                const bool ok = g0.y == tag && g0.w == tag && g1.y == tag && g1.w == tag && g2.y == tag && g2.w == tag && g3.y == tag && g3.w == tag;
                if (__all(ok) || dead) break;
                if (spins > SPIN_CAP) { dead = true; break; }
                __builtin_amdgcn_s_sleep(4);
            }
            *(LAS v4u*)(L + L_QF + lane * 16) = v4u{g0.x, g0.z, g1.x, g1.z};
            *(LAS v4u*)(L + L_QF + 1024 + lane * 16) = v4u{g2.x, g2.z, g3.x, g3.z};
            asm volatile("s_waitcnt lgkmcnt(0)" ::: "memory");
            if (lane == 0) flags[0] = dead ? 2u : 1u;
        }
        STAMP(4);
        {
            const GAS v4u* xp = (const GAS v4u*)p.x; const GAS v4u* gp = (const GAS v4u*)p.gw; const GAS v4u* rp = (const GAS v4u*)p.wr;
            float sg = 0.f, sr = 0.f;
#pragma unroll
            for (int i = 0; i < 2; ++i) {
                const v4u xv = xp[lane + 64 * i], gv = gp[lane + 64 * i], rv = rp[lane + 64 * i];
#pragma unroll
                for (int k = 0; k < 4; ++k) {
                    sg += rbf(bflo(xv[k]) * bflo(gv[k])); sg += rbf(bfhi(xv[k]) * bfhi(gv[k]));
                    sr = fmaf(bflo(xv[k]), bflo(rv[k]), sr); sr = fmaf(bfhi(xv[k]), bfhi(rv[k]), sr);
                }
            }
            sg = rbf(wave_sum(sg)); sr = rbf(wave_sum(sr));
            const float g_gate = rbf(1.f / (1.f + __expf(-sg)));
            const float rsg = rbf(1.f / (1.f + __expf(-sr)));
            if (lane == 0) *(LAS f32x2*)(L + L_SC) = f32x2{g_gate, (1.f - g_gate) * rsg};
        }
        lds_wait_slow(flags, 6, 1u);
        {
            const int xo = WS_H + w * H_STRIDE;
            const bool used = (lane & 15) < 14;
            v4u d[8];
            for (unsigned spins = 0;; ++spins) {
                asm volatile("" ::: "memory");
#pragma unroll
                for (int i = 0; i < 8; ++i) d[i] = ld_sc1(rs, xo + i * 1024 + lane * 16);
                bool ok = true;
#pragma unroll
                for (int i = 0; i < 8; ++i) ok = ok && d[i].y == tag && d[i].w == tag;
                if (__all(ok || !used) || dead) break;
                if (spins > SPIN_CAP) { dead = true; break; }
                __builtin_amdgcn_s_sleep(32);
            }
            float s0 = 0.f, s1 = 0.f;
#pragma unroll
            for (int i = 0; i < 8; ++i) { s0 += uf(d[i].x); s1 += uf(d[i].z); }
            s0 = rbf(x16x32_sum(s0)); s1 = rbf(x16x32_sum(s1));
            const float u0 = __shfl(s0, (lane & 15) + 7), u1 = __shfl(s1, (lane & 15) + 7);
            const float a0 = rbf(s0 / (1.f + __expf(-s0))), a1 = rbf(s1 / (1.f + __expf(-s1)));
            float h0 = rbf(a0 * u0), h1 = rbf(a1 * u1);
            if (lane >= 7) { h0 = 0.f; h1 = 0.f; }
            if (dead) h0 = __builtin_nanf("");
            if (lane < 8) *(LAS f32x2*)(L + L_HV + lane * 8) = f32x2{h0, h1};
        }
        STAMP(5);
        WG_BARRIER();
        STAMP_FLUSH();
        return;
    }

    const int grow = 32 * rb + 4 * wv;
    const unsigned char* wq_p = (const unsigned char*)p.wq + (size_t)grow * (NQ * 2) + cb * 1024 + lane * 16;
    const size_t kv_base = ((size_t)kvh * SK + (size_t)sp * 1024 + wv * 4) * 256;
    const unsigned char* k_p = (const unsigned char*)p.kc + kv_base + ((lane >> 2) & 3) * 256 + (4 * (lane >> 4) + (lane & 3)) * 16;
    const unsigned char* v_p = (const unsigned char*)p.vc + kv_base + lane * 16;
    const int glane = lane < 56 ? lane : 55;
    const size_t goff = (size_t)grow * (FF * 2) + cb * 896 + glane * 16;
    const unsigned char* wg_p = (const unsigned char*)p.wg + goff;
    const unsigned char* wu_p = (const unsigned char*)p.wu + goff;
    const unsigned char* wd_p = (const unsigned char*)p.wd + (size_t)(w * 14) * (DM * 2) + wv * 256 + (lane & 15) * 16;
    const unsigned char* wo_p = (const unsigned char*)p.wo + (size_t)(w * 16 + (lane >> 4)) * (DM * 2) + wv * 256 + (lane & 15) * 16;

    auto issue_w = [&](int i) -> v4u {
        if (i < 4) return ldnt(wq_p + (size_t)i * (NQ * 2));
        if (i < 8) return ldnt(wg_p + (size_t)(i - 4) * (FF * 2));
        if (i < 12) return ldnt(wu_p + (size_t)(i - 8) * (FF * 2));
        if (i < 16) { const int r = 4 * (i - 12) + (lane >> 4); return ldnt(wd_p + (size_t)(r < 14 ? r : 13) * (DM * 2)); }
        return ldnt(wo_p + (size_t)(i - 16) * 4 * (DM * 2));
    };
    auto issue = [&](int e) -> v4u {
        const int r = e / (2 * TPR), k = e % (2 * TPR);
        return k < TPR ? ldnt(k_p + (size_t)(r * TPR + k) * 8192) : ldnt(v_p + (size_t)(r * TPR + k - TPR) * 8192);
    };
    v4u ring[DT];
#define ISSUE(j) do { THROTTLE(); ring[(j) % DT] = issue(j); SB(); } while (0)
#define AFTER(e) do { if (jmax(e) > ((e) == 0 ? DT - 1 : jmax((e) - 1))) ISSUE(jmax(e)); } while (0)
#define GET(e) ring[(e) % DT]

    const v2u xg = *(const GAS v2u*)(p.x + grow);
    v4u wt[12];
#pragma unroll
    for (int i = 0; i < 4; ++i) wt[i] = issue_w(i);
    SB();
    WG_BARRIER();
#pragma unroll
    for (int i = 4; i < 4 + EARLY; ++i) wt[i] = issue_w(i);
    SB();
    bool dead = false;
    {
        const float xv[4] = {bflo(xg.x), bfhi(xg.x), bflo(xg.y), bfhi(xg.y)};
        float acc[8] = {0.f, 0.f, 0.f, 0.f, 0.f, 0.f, 0.f, 0.f};
#pragma unroll
        for (int t = 0; t < 4; ++t) fma8(acc, wt[t], xv[t]);
        {
            LAS unsigned char* q = L + L_QP + (wv * 512 + lane * 8) * 4;
            *(LAS f32x4*)(q) = f32x4{acc[0], acc[1], acc[2], acc[3]}; *(LAS f32x4*)(q + 16) = f32x4{acc[4], acc[5], acc[6], acc[7]};
        }
        lds_arrive(L, 1, lane);
        STAMP(1);
        float ag[8] = {0.f, 0.f, 0.f, 0.f, 0.f, 0.f, 0.f, 0.f}, au[8] = {0.f, 0.f, 0.f, 0.f, 0.f, 0.f, 0.f, 0.f};
#pragma unroll
        for (int t = 4 + EARLY; t < 12; ++t) { THROTTLE(); wt[t] = issue_w(t); SB(); }
#pragma unroll
        for (int j = 0; j < 4; ++j) ISSUE(j);
#pragma unroll
        for (int t = 0; t < 4; ++t) { fma8(ag, wt[4 + t], xv[t]); }
#pragma unroll
        for (int t = 0; t < 4; ++t) { fma8(au, wt[8 + t], xv[t]); }
        if (lane < 56) {
            LAS unsigned char* q = L + L_GU + (wv * 2 * 448 + lane * 8) * 4;
            *(LAS f32x4*)(q) = f32x4{ag[0], ag[1], ag[2], ag[3]}; *(LAS f32x4*)(q + 16) = f32x4{ag[4], ag[5], ag[6], ag[7]};
            *(LAS f32x4*)(q + 1792) = f32x4{au[0], au[1], au[2], au[3]}; *(LAS f32x4*)(q + 1792 + 16) = f32x4{au[4], au[5], au[6], au[7]};
        }
        lds_arrive(L, 2, lane);
    }
#pragma unroll
    for (int j = 4; j < DT; ++j) {
        ISSUE(j);
        if (j == PROG1 && wv == 0 && lane == 0) flags[4] = 1u;
        if (j == PROG2 && wv == 0 && lane == 0) flags[5] = 1u;
    }
    STAMP(2);
    lds_wait(flags, 0, 1u);
    if (flags[0] != 1u) dead = true;
    const v4u qf0 = *(const LAS v4u*)(L + L_QF + lane * 16), qf1 = *(const LAS v4u*)(L + L_QF + 1024 + lane * 16);
    STAMP(3);

    constexpr float C2 = 0.088388347648318440f * 1.4426950408889634f;
    const unsigned mk0 = (lane & 3) == 0 ? 0x0000ffffu : (lane & 3) == 1 ? 0xffff0000u : 0u;
    const unsigned mk1 = (lane & 3) == 2 ? 0x0000ffffu : (lane & 3) == 3 ? 0xffff0000u : 0u;
    const int sub = lane & 3;
    f32x4 o00 = {0.f, 0.f, 0.f, 0.f}, o01 = o00, o10 = o00, o11 = o00;
    float m0 = 0.f, m1 = 0.f, l0 = 0.f, l1 = 0.f;
#pragma unroll
    for (int r = 0; r < NROUND; ++r) {
        const int base = r * 2 * TPR;
        const f32x4 z = {0.f, 0.f, 0.f, 0.f};
        f32x4 d0[TPR], d1[TPR];
        float P0, P1;
        d0[0] = mfma32(GET(base + 0), qf0, z); d1[0] = mfma32(GET(base + 0), qf1, z); AFTER(base + 0);
        d0[1] = mfma32(GET(base + 1), qf0, z); d1[1] = mfma32(GET(base + 1), qf1, z); AFTER(base + 1);
        d0[2] = mfma32(GET(base + 2), qf0, z); d1[2] = mfma32(GET(base + 2), qf1, z);
        P0 = qred(d0[0]); P1 = qred(d1[0]);
        AFTER(base + 2);
        d0[3] = mfma32(GET(base + 3), qf0, z); d1[3] = mfma32(GET(base + 3), qf1, z);
        { const float a = qred(d0[1]), c = qred(d1[1]); P0 = sub == 1 ? a : P0; P1 = sub == 1 ? c : P1; }
        AFTER(base + 3);
        { const float a = qred(d0[2]), c = qred(d1[2]); P0 = sub == 2 ? a : P0; P1 = sub == 2 ? c : P1; }
        { const float a = qred(d0[3]), c = qred(d1[3]); P0 = sub == 3 ? a : P0; P1 = sub == 3 ? c : P1; }
        float t0 = fmaxf(P0, dpp<XOR1>(P0)), t1 = fmaxf(P1, dpp<XOR1>(P1));
        t0 = fmaxf(t0, dpp<XOR2>(t0)); t1 = fmaxf(t1, dpp<XOR2>(t1));
        t0 = x16x32_max(t0) * C2; t1 = x16x32_max(t1) * C2;
        if (r == 0) { m0 = t0; m1 = t1; }
        else if (__any(t0 > m0 || t1 > m1)) {
            const float n0 = fmaxf(m0, t0), n1 = fmaxf(m1, t1);
            const float a0 = __builtin_amdgcn_exp2f(m0 - n0), a1 = __builtin_amdgcn_exp2f(m1 - n1);
            m0 = n0; m1 = n1; l0 *= a0; l1 *= a1;
            const float b0 = __shfl(a0, (lane >> 4) * 20), b1 = __shfl(a1, (lane >> 4) * 20);
            o00 *= b0; o01 *= b0; o10 *= b1; o11 *= b1;
        }
        const float p0 = __builtin_amdgcn_exp2f(fmaf(P0, C2, -m0)), p1 = __builtin_amdgcn_exp2f(fmaf(P1, C2, -m1));
        l0 += p0; l1 += p1;
        const unsigned pp0 = pk(p0, p0), pp1 = pk(p1, p1);
        if (PRIO && r == 0 && wv >= 4) __builtin_amdgcn_s_setprio(1);
#define PV(t) do { \
            const unsigned b0_ = qbc<t>(pp0), b1_ = qbc<t>(pp1); \
            const v2u a0_ = {b0_ & mk0, b0_ & mk1}, a1_ = {b1_ & mk0, b1_ & mk1}; \
            const v4u vf_ = GET(base + TPR + (t)); \
            o00 = mfma16(a0_, v2u{vf_.x, vf_.y}, o00); o01 = mfma16(a0_, v2u{vf_.z, vf_.w}, o01); \
            o10 = mfma16(a1_, v2u{vf_.x, vf_.y}, o10); o11 = mfma16(a1_, v2u{vf_.z, vf_.w}, o11); \
            AFTER(base + TPR + (t)); } while (0)
        PV(0); PV(1); PV(2); PV(3);
        if (r == NROUND / 2 - 1) { STAMP(4); if (wv == 0 && lane == 0) flags[6] = 1u; }
    }
    STAMP(5);
    l0 += dpp<XOR1>(l0); l1 += dpp<XOR1>(l1); l0 += dpp<XOR2>(l0); l1 += dpp<XOR2>(l1);
    l0 = x16x32_sum(l0); l1 = x16x32_sum(l1);
    {
        LAS unsigned char* mb = L + L_MRG + wv * 4096 + (lane >> 4) * 512 + (lane & 15) * 32;
        *(LAS f32x4*)(mb) = o00; *(LAS f32x4*)(mb + 16) = o01;
        *(LAS f32x4*)(mb + 2048) = o10; *(LAS f32x4*)(mb + 2048 + 16) = o11;
        if ((lane & 0x33) == 0) {
            LAS unsigned char* q = L + L_ML + wv * 64 + (lane >> 2) * 8;
            *(LAS f32x2*)(q) = f32x2{m0, dead ? __builtin_nanf("") : l0}; *(LAS f32x2*)(q + 32) = f32x2{m1, l1};
        }
    }
    WG_BARRIER();
    STAMP(6);
    const unsigned tag = flags[3];
    {
        f32x2 ml[NSW];
#pragma unroll
        for (int i = 0; i < NSW; ++i) ml[i] = *(const LAS f32x2*)(L + L_ML + i * 64 + wv * 8);
        float M = ml[0][0];
#pragma unroll
        for (int i = 1; i < NSW; ++i) M = fmaxf(M, ml[i][0]);
        float Ls = 0.f, a0 = 0.f, a1 = 0.f;
#pragma unroll
        for (int i = 0; i < NSW; ++i) {
            const float wt_ = __builtin_amdgcn_exp2f(ml[i][0] - M);
            const f32x2 o = *(const LAS f32x2*)(L + L_MRG + i * 4096 + wv * 512 + lane * 8);
            Ls = fmaf(wt_, ml[i][1], Ls); a0 = fmaf(wt_, o[0], a0); a1 = fmaf(wt_, o[1], a1);
        }
        st_sc1(rs, WS_X1 + (kvh * 64 + wv * 8 + (lane >> 3)) * X1_STRIDE + sp * 128 + (lane & 7) * 16, v4u{fu(a0), tag, fu(a1), tag});
        if (lane == 0) st_sc1(rs, WS_ML + ((kvh * 8 + wv) * 64 + sp) * 16, v4u{fu(M), tag, fu(Ls), tag});
    }
    STAMP(7);
    const f32x2 sc = *(const LAS f32x2*)(L + L_SC);
    f32x4 acc[2] = {{0.f, 0.f, 0.f, 0.f}, {0.f, 0.f, 0.f, 0.f}};
    v4u tl[8];
    {
        const int xo = WS_X1 + w * X1_STRIDE + wv * 1024 + lane * 16;
        const int mo = WS_ML + ((w >> 3) * 64 + wv * 8 + (lane >> 3)) * 16;
        v4u ml = ld_sc1(rs, mo), d = ld_sc1(rs, xo);
        SB();
#pragma unroll
        for (int t = 0; t < 8; ++t) { tl[t] = issue_w(12 + t); }
        SB();
        for (unsigned spins = 0;; ++spins) {
            if (__all(ml.y == tag && ml.w == tag && d.y == tag && d.w == tag)) break;
            if (spins > SPIN_CAP) { dead = true; break; }
            asm volatile("" ::: "memory");
            ml = ld_sc1(rs, mo); d = ld_sc1(rs, xo);
        }
        const float m = uf(ml.x);
        float M = fmaxf(m, dpp<XOR8>(m)); M = x16x32_max(M);
        const float wt_ = __builtin_amdgcn_exp2f(m - M);
        float Ls = wt_ * uf(ml.z), o0 = wt_ * uf(d.x), o1 = wt_ * uf(d.z);
        Ls += dpp<XOR8>(Ls); o0 += dpp<XOR8>(o0); o1 += dpp<XOR8>(o1);
        Ls = x16x32_sum(Ls); o0 = x16x32_sum(o0); o1 = x16x32_sum(o1);
        if (lane < 8) {
            *(LAS float*)(L + L_X1 + 64 + ((2 * lane) * 8 + wv) * 4) = o0;
            *(LAS float*)(L + L_X1 + 64 + ((2 * lane + 1) * 8 + wv) * 4) = o1;
            if (lane == 0) *(LAS f32x2*)(L + L_X1 + wv * 8) = f32x2{M, dead ? __builtin_nanf("") : Ls};
        }
#pragma unroll
        for (int t = 0; t < 4; ++t) {
            const float h = *(const LAS float*)(L + L_HV + (4 * t + (lane >> 4)) * 4) * sc[1];
            rows_mfma(acc, tl[t], pk(h, h), mk0, mk1);
        }
    }
    STAMP(8);
    WG_BARRIER();
    {
        f32x2 ml[NSW];
#pragma unroll
        for (int i = 0; i < NSW; ++i) ml[i] = *(const LAS f32x2*)(L + L_X1 + i * 8);
        float M = ml[0][0];
#pragma unroll
        for (int i = 1; i < NSW; ++i) M = fmaxf(M, ml[i][0]);
        float wt_[NSW], Ls = 0.f;
#pragma unroll
        for (int i = 0; i < NSW; ++i) { wt_[i] = __builtin_amdgcn_exp2f(ml[i][0] - M); Ls = fmaf(wt_[i], ml[i][1], Ls); }
        const float inv = 1.f / Ls;
#pragma unroll
        for (int t = 0; t < 4; ++t) {
            const LAS unsigned char* q = L + L_X1 + 64 + (4 * t + (lane >> 4)) * 32;
            const f32x4 u = *(const LAS f32x4*)(q), v = *(const LAS f32x4*)(q + 16);
            float o = wt_[0] * u[0];
            o = fmaf(wt_[1], u[1], o); o = fmaf(wt_[2], u[2], o); o = fmaf(wt_[3], u[3], o);
            o = fmaf(wt_[4], v[0], o); o = fmaf(wt_[5], v[1], o); o = fmaf(wt_[6], v[2], o); o = fmaf(wt_[7], v[3], o);
            o = rbf(o * inv) * sc[0];
            rows_mfma(acc, tl[4 + t], pk(o, o), mk0, mk1);
        }
        const int g2 = lane >> 4, c = lane & 15;
        const f32x4 a = (g2 & 2) ? acc[1] : acc[0];
        const float v0 = (g2 & 1) ? a[2] : a[0], v1 = (g2 & 1) ? a[3] : a[1];
        const int cons = 8 * wv + (c >> 1);
        st_sc1(rs, WS_X2 + cons * X2_STRIDE + w * 128 + ((c & 1) * 4 + g2) * 16, v4u{fu(v0), tag, fu(v1), tag});
    }
    STAMP(9);
    if ((w & 3) == 0) {
        const int xo = WS_X2 + (w >> 2) * X2_STRIDE + wv * 4096 + lane * 16;
        const unsigned xv = *(const GAS unsigned*)(p.x + 16 * (w >> 2) + 2 * (lane & 7));
        v4u d[4];
#pragma unroll
        for (int i = 0; i < 4; ++i) d[i] = ld_sc1(rs, xo + i * 1024);
        for (unsigned spins = 0;; ++spins) {
            unsigned pend = 0u;
#pragma unroll
            for (int i = 0; i < 4; ++i) if (!__all(d[i].y == tag && d[i].w == tag)) pend |= 1u << i;
            if (pend == 0u) break;
            if (spins > SPIN_CAP) { dead = true; break; }
            asm volatile("" ::: "memory");
#pragma unroll
            for (int i = 0; i < 4; ++i) if ((pend >> i) & 1u) d[i] = ld_sc1(rs, xo + i * 1024);
        }
        float s0 = (uf(d[0].x) + uf(d[1].x)) + (uf(d[2].x) + uf(d[3].x)), s1 = (uf(d[0].z) + uf(d[1].z)) + (uf(d[2].z) + uf(d[3].z));
        s0 += dpp<XOR8>(s0); s1 += dpp<XOR8>(s1);
        s0 = x16x32_sum(s0); s1 = x16x32_sum(s1);
        if (dead) s0 = __builtin_nanf("");
        if (lane < 8) *(LAS f32x2*)(L + L_X2 + wv * 64 + lane * 8) = f32x2{s0, s1};
        STAMP(10);
        WG_BARRIER();
        if (wv == 0 && lane < 8) {
            f32x2 s = {0.f, 0.f};
#pragma unroll
            for (int i = 0; i < NSW; ++i) s += *(const LAS f32x2*)(L + L_X2 + i * 64 + lane * 8);
            const int col = 16 * (w >> 2) + 2 * lane;
            *(GAS unsigned*)(p.out + col) = pk(fmaf(1.f - sc[0], bflo(xv), s[0]), fmaf(1.f - sc[0], bfhi(xv), s[1]));
            if (w == 0 && lane == 0) __builtin_amdgcn_raw_buffer_store_b32(tag, rs, WS_EPOCH, 0, AUX_SC1);
        }
        STAMP(11);
    }
    STAMP_FLUSH();
}

extern "C" void kernel_launch(void* const* d_in, const int* in_sizes, int n_in, void* d_out, int out_size, void* d_ws, size_t ws_size,
                              hipStream_t stream) {
    Params p;
    p.x = (const unsigned short*)d_in[0];
    p.kc = (const unsigned short*)d_in[1];
    p.vc = (const unsigned short*)d_in[2];
    p.wq = (const unsigned short*)d_in[3];
    p.wo = (const unsigned short*)d_in[4];
    p.wr = (const unsigned short*)d_in[6];
    p.wg = (const unsigned short*)d_in[7];
    p.wu = (const unsigned short*)d_in[8];
    p.wd = (const unsigned short*)d_in[9];
    p.gw = (const unsigned short*)d_in[10];
    p.out = (unsigned short*)d_out;
    p.ws = (unsigned char*)d_ws;
    (void)hipFuncSetAttribute((const void*)fused_decode_kernel, hipFuncAttributeMaxDynamicSharedMemorySize, LDS_BYTES);
    hipLaunchKernelGGL(fused_decode_kernel, dim3(NWG), dim3(NTHR), LDS_BYTES, stream, p);
}
```

```cpp
#include <hip/hip_runtime.h>
#include <hip/hip_bf16.h>
#include <stdint.h>

#define GAS __attribute__((address_space(1)))
#define LAS __attribute__((address_space(3)))

typedef unsigned v4u __attribute__((ext_vector_type(4)));
typedef unsigned v2u __attribute__((ext_vector_type(2)));
typedef float f32x4 __attribute__((ext_vector_type(4)));
typedef float f32x2 __attribute__((ext_vector_type(2)));
typedef short bf16x8 __attribute__((ext_vector_type(8)));
typedef short bf16x4 __attribute__((ext_vector_type(4)));
typedef __bf16 bf2 __attribute__((ext_vector_type(2)));

namespace {

constexpr int NWG = 256, NSW = 8, NTHR = (NSW + 1) * 64;
constexpr int DM = 1024, SK = 65536, DH = 128, FF = 3584, NQ = 4096;
#define EARLY 2
#define PRIO 0
#define DEPTH 4
#define DTW 28
#define PROG1 4
#define PROG2 13
constexpr int TPR = 4, NROUND = 8;
constexpr int E_WD = 64, E_WO = 68, E_END = 72;
constexpr int DT = DTW;
constexpr int jmax(int e) {
    const int a = DT - 1 + (e + 2) / 2, b = e + DEPTH + 1;
    const int m = a > b ? a : b;
    return m < E_END - 1 ? m : E_END - 1;
}

constexpr int WS_EPOCH = 0;
constexpr int WS_Q = 4096;
constexpr int WS_Q1 = WS_Q + 2048 * 8;
constexpr int Q1_STRIDE = 32 * 128;
constexpr int WS_ML = WS_Q1 + NWG * Q1_STRIDE;
constexpr int WS_X1 = WS_ML + 32 * 64 * 16;
constexpr int X1_STRIDE = 64 * 128;
constexpr int WS_X2 = WS_X1 + NWG * X1_STRIDE;
constexpr int X2_STRIDE = NWG * 128;
constexpr int WS_H = WS_X2 + 64 * X2_STRIDE;
constexpr int H_STRIDE = 32 * 256;
constexpr int WS_END = WS_H + NWG * H_STRIDE;

constexpr int L_FLAGS = 0;
constexpr int L_HV = 128;
constexpr int L_SC = 192;
constexpr int L_QF = 256;
constexpr int L_ML = L_QF + 2048;
constexpr int L_X1 = L_ML + 512;
constexpr int L_X2 = L_X1 + 64 + 512;
constexpr int L_GU = L_X2 + 512;
constexpr int L_QP = L_GU + 8 * 2 * 448 * 4;
constexpr int L_MRG = L_QP + 8 * 512 * 4;
constexpr int L_END = L_MRG + 8 * 8 * 128 * 4;
constexpr int LDS_BYTES = 88 * 1024;
static_assert(L_END <= LDS_BYTES && LDS_BYTES > 80 * 1024, "lds");
static_assert(L_MRG % 16 == 0 && L_GU % 16 == 0 && L_QP % 16 == 0 && L_X1 % 16 == 0 && L_X2 % 16 == 0, "align");

constexpr unsigned SPIN_CAP = 1u << 18;
constexpr int AUX_SC1 = 16;

struct Params {
    const unsigned short* x;
    const unsigned short* kc;
    const unsigned short* vc;
    const unsigned short* wq;
    const unsigned short* wo;
    const unsigned short* wr;
    const unsigned short* wg;
    const unsigned short* wu;
    const unsigned short* wd;
    const unsigned short* gw;
    unsigned short* out;
    unsigned char* ws;
};

__device__ __forceinline__ v4u ldnt(const unsigned char* p) { return __builtin_nontemporal_load((const GAS v4u*)p); }
__device__ __forceinline__ float bflo(unsigned u) { return __builtin_bit_cast(float, u << 16); }
__device__ __forceinline__ float bfhi(unsigned u) { return __builtin_bit_cast(float, u & 0xffff0000u); }
__device__ __forceinline__ unsigned pk(float lo, float hi) {
    bf2 r = {(__bf16)lo, (__bf16)hi};
    return __builtin_bit_cast(unsigned, r);
}
__device__ __forceinline__ float rbf(float v) { return bflo(pk(v, v)); }
__device__ __forceinline__ unsigned fu(float f) { return __builtin_bit_cast(unsigned, f); }
__device__ __forceinline__ float uf(unsigned u) { return __builtin_bit_cast(float, u); }
template <int CTRL> __device__ __forceinline__ float dpp(float x) {
    return uf((unsigned)__builtin_amdgcn_mov_dpp((int)fu(x), CTRL, 0xf, 0xf, true));
}
constexpr int XOR1 = 0xB1, XOR2 = 0x4E, XOR7 = 0x141, XOR8 = 0x128, ROR4 = 0x124;
__device__ __forceinline__ float swap32_add(float a, float b) {
    auto s = __builtin_amdgcn_permlane32_swap(fu(a), fu(b), false, false);
    return uf(s[0]) + uf(s[1]);
}
__device__ __forceinline__ float swap16_add(float a, float b) {
    auto s = __builtin_amdgcn_permlane16_swap(fu(a), fu(b), false, false);
    return uf(s[0]) + uf(s[1]);
}
__device__ __forceinline__ float x16x32_max(float x) {
    auto s = __builtin_amdgcn_permlane16_swap(fu(x), fu(x), false, false);
    x = fmaxf(uf(s[0]), uf(s[1]));
    auto t = __builtin_amdgcn_permlane32_swap(fu(x), fu(x), false, false);
    return fmaxf(uf(t[0]), uf(t[1]));
}
__device__ __forceinline__ float x16x32_sum(float x) { x = swap16_add(x, x); return swap32_add(x, x); }
__device__ __forceinline__ float row_sum(float x) {
    x += dpp<XOR1>(x); x += dpp<XOR2>(x); x += dpp<XOR7>(x); x += dpp<XOR8>(x); return x;
}
__device__ __forceinline__ float row_max(float x) {
    x = fmaxf(x, dpp<XOR1>(x)); x = fmaxf(x, dpp<XOR2>(x)); x = fmaxf(x, dpp<XOR7>(x)); x = fmaxf(x, dpp<XOR8>(x)); return x;
}
__device__ __forceinline__ float wave_sum(float x) { return x16x32_sum(row_sum(x)); }
__device__ __forceinline__ float wave_max(float x) { return x16x32_max(row_max(x)); }

__device__ __forceinline__ f32x4 mfma32(v4u a, v4u b, f32x4 c) {
    return __builtin_amdgcn_mfma_f32_16x16x32_bf16(__builtin_bit_cast(bf16x8, a), __builtin_bit_cast(bf16x8, b), c, 0, 0, 0);
}
__device__ __forceinline__ f32x4 mfma16(v2u a, v2u b, f32x4 c) {
    return __builtin_amdgcn_mfma_f32_16x16x16bf16_1k(__builtin_bit_cast(bf16x4, a), __builtin_bit_cast(bf16x4, b), c, 0, 0, 0);
}
#define WG_BARRIER() do { asm volatile("s_waitcnt lgkmcnt(0)" ::: "memory"); __builtin_amdgcn_s_barrier(); asm volatile("" ::: "memory"); } while (0)
#define SB() __builtin_amdgcn_sched_barrier(0)
#define SBMASK 0x10F
#define SBM() __builtin_amdgcn_sched_barrier(SBMASK)
#define STR2(x) #x
#define STR(x) STR2(x)
#define THROTTLE() asm volatile("s_waitcnt vmcnt(" STR(DEPTHM1) ")" ::: "memory")
#define DEPTHM1 3

__device__ __forceinline__ void fma8(float (&acc)[8], v4u wv, float xv) {
    acc[0] = fmaf(xv, bflo(wv.x), acc[0]); acc[1] = fmaf(xv, bfhi(wv.x), acc[1]);
    acc[2] = fmaf(xv, bflo(wv.y), acc[2]); acc[3] = fmaf(xv, bfhi(wv.y), acc[3]);
    acc[4] = fmaf(xv, bflo(wv.z), acc[4]); acc[5] = fmaf(xv, bfhi(wv.z), acc[5]);
    acc[6] = fmaf(xv, bflo(wv.w), acc[6]); acc[7] = fmaf(xv, bfhi(wv.w), acc[7]);
}
__device__ __forceinline__ f32x2 reduce_cols(const float (&a)[8]) {
    const float b0 = swap32_add(a[0], a[4]), b1 = swap32_add(a[1], a[5]), b2 = swap32_add(a[2], a[6]), b3 = swap32_add(a[3], a[7]);
    float c0 = swap16_add(b0, b2), c1 = swap16_add(b1, b3);
    c0 += dpp<XOR2>(c0); c1 += dpp<XOR2>(c1);
    c0 += dpp<ROR4>(c0); c1 += dpp<ROR4>(c1);
    c0 += dpp<XOR8>(c0); c1 += dpp<XOR8>(c1);
    return f32x2{c0, c1};
}

__device__ __forceinline__ v4u ld_sc1(__amdgpu_buffer_rsrc_t rs, int off) { return __builtin_amdgcn_raw_buffer_load_b128(rs, off, 0, AUX_SC1); }
__device__ __forceinline__ void st_sc1(__amdgpu_buffer_rsrc_t rs, int off, v4u v) { __builtin_amdgcn_raw_buffer_store_b128(v, rs, off, 0, AUX_SC1); }

__device__ __forceinline__ void rows_mfma(f32x4 (&acc)[2], v4u wv, unsigned pp, unsigned m0, unsigned m1) {
    const v2u a = {pp & m0, pp & m1};
    acc[0] = mfma16(a, v2u{wv.x, wv.y}, acc[0]);
    acc[1] = mfma16(a, v2u{wv.z, wv.w}, acc[1]);
}
__device__ __forceinline__ unsigned lds_arrive(LAS unsigned char* L, int word, int lane) {
    asm volatile("s_waitcnt lgkmcnt(0)" ::: "memory");
    unsigned old = 0;
    if (lane == 0) old = __hip_atomic_fetch_add((LAS unsigned*)(L + L_FLAGS + 4 * word), 1u, __ATOMIC_RELAXED, __HIP_MEMORY_SCOPE_WORKGROUP);
    old = __builtin_amdgcn_readfirstlane(old);
    asm volatile("" ::: "memory");
    return old;
}
__device__ __forceinline__ void lds_wait_slow(volatile LAS unsigned* flags, int word, unsigned want) {
    for (unsigned spins = 0; flags[word] < want && spins < SPIN_CAP; ++spins) __builtin_amdgcn_s_sleep(8);
    asm volatile("" ::: "memory");
}
__device__ __forceinline__ void lds_wait(volatile LAS unsigned* flags, int word, unsigned want) {
    for (unsigned spins = 0; flags[word] < want && spins < (SPIN_CAP << 4); ++spins) __builtin_amdgcn_s_sleep(1);
    asm volatile("" ::: "memory");
}
__device__ __forceinline__ float qred(f32x4 d) {
    float t = dpp<0x00>(d[0]);
    t = dpp<0x55>(d[1]) + t;
    asm volatile("" : "+v"(t));
    t = dpp<0xAA>(d[2]) + t;
    t = dpp<0xFF>(d[3]) + t;
    return t;
}
template <int T> __device__ __forceinline__ unsigned qbc(unsigned x) {
    return (unsigned)__builtin_amdgcn_mov_dpp((int)x, T * 0x55, 0xf, 0xf, true);
}

#define STAMP(i) do {} while (0)
#define STAMP_DECL() do {} while (0)
#define STAMP_FLUSH() do {} while (0)

}

__global__ void __launch_bounds__(NTHR, 1) fused_decode_kernel(Params p) {
    extern __shared__ __attribute__((aligned(16))) unsigned char smem[];
    LAS unsigned char* L = (LAS unsigned char*)smem;
    volatile LAS unsigned* flags = (volatile LAS unsigned*)(L + L_FLAGS);
    const int tid = threadIdx.x, lane = tid & 63;
    const int wv = __builtin_amdgcn_readfirstlane(tid >> 6);
    const int b = blockIdx.x;
    const int w = (b & 7) * 32 + (b >> 3);
    const int kvh = w >> 6, sp = w & 63;
    const int cb = w & 7, rb = w >> 3;
    const __amdgpu_buffer_rsrc_t rs = __builtin_amdgcn_make_buffer_rsrc((void*)p.ws, 0, WS_END, 0x00020000);
    STAMP_DECL();
    STAMP(0);

    if (wv == NSW) {
        const unsigned ep = __builtin_amdgcn_raw_buffer_load_b32(rs, WS_EPOCH, 0, AUX_SC1);
        if (lane < 8) flags[lane] = 0u;
        WG_BARRIER();
        const unsigned tag = __builtin_amdgcn_readfirstlane(ep) + 1u;
        if (lane == 0) flags[3] = tag;
        bool dead = false;
        STAMP(1);
        lds_wait(flags, 1, NSW);
#pragma unroll
        for (int it = 0; it < 4; ++it) {
            const int c = it * 128 + lane * 2;
            f32x2 s = {0.f, 0.f};
#pragma unroll
            for (int i = 0; i < NSW; ++i) s += *(const LAS f32x2*)(L + L_QP + (i * 512 + c) * 4);
            st_sc1(rs, WS_Q1 + (cb * 32 + (c >> 4)) * Q1_STRIDE + rb * 128 + (c & 15) * 8, v4u{fu(s[0]), tag, fu(s[1]), tag});
        }
        STAMP(2);
        lds_wait_slow(flags, 4, 1u);
        {
            const int xo = WS_Q1 + w * Q1_STRIDE;
            v4u d[4];
            for (unsigned spins = 0;; ++spins) {
                asm volatile("" ::: "memory");
#pragma unroll
                for (int i = 0; i < 4; ++i) d[i] = ld_sc1(rs, xo + i * 1024 + lane * 16);
                bool ok = true;
#pragma unroll
                for (int i = 0; i < 4; ++i) ok = ok && d[i].y == tag && d[i].w == tag;
                if (__all(ok)) break;
                if (spins > SPIN_CAP) { dead = true; break; }
                __builtin_amdgcn_s_sleep(8);
            }
            float s0 = (uf(d[0].x) + uf(d[1].x)) + (uf(d[2].x) + uf(d[3].x)), s1 = (uf(d[0].z) + uf(d[1].z)) + (uf(d[2].z) + uf(d[3].z));
            s0 += dpp<XOR8>(s0); s1 += dpp<XOR8>(s1);
            s0 = x16x32_sum(s0); s1 = x16x32_sum(s1);
            const unsigned pr = pk(s0, s1);
            const unsigned nx = __builtin_amdgcn_mov_dpp((int)pr, XOR1, 0xf, 0xf, true);
            if (lane < 8 && !(lane & 1) && !dead) st_sc1(rs, WS_Q + (w * 8 + lane) * 8, v4u{pr, tag, nx, tag});
        }
        STAMP(3);
        lds_wait(flags, 2, NSW);
#pragma unroll
        for (int it = 0; it < 7; ++it) {
            const int idx = it * 64 + lane, j = idx / 14, k = idx - 14 * j;
            const int mat = k >= 7 ? 1 : 0, u = 14 * j + 2 * (k - 7 * mat);
            f32x2 s = {0.f, 0.f};
#pragma unroll
            for (int i = 0; i < NSW; ++i) s += *(const LAS f32x2*)(L + L_GU + ((i * 2 + mat) * 448 + u) * 4);
            st_sc1(rs, WS_H + (cb * 32 + j) * H_STRIDE + rb * 256 + k * 16, v4u{fu(s[0]), tag, fu(s[1]), tag});
        }
        lds_wait_slow(flags, 5, 1u);
        {
            const int qoff = WS_Q + (kvh * 512 + ((lane >> 2) & 3) * 64 + (4 * (lane >> 4) + (lane & 3)) * 4) * 8;
            v4u g0, g1, g2, g3;
            for (unsigned spins = 0;; ++spins) {
                asm volatile("" ::: "memory");
                g0 = ld_sc1(rs, qoff); g1 = ld_sc1(rs, qoff + 16); g2 = ld_sc1(rs, qoff + 2048); g3 = ld_sc1(rs, qoff + 2048 + 16);
                const bool ok = g0.y == tag && g0.w == tag && g1.y == tag && g1.w == tag && g2.y == tag && g2.w == tag && g3.y == tag && g3.w == tag;
                if (__all(ok) || dead) break;
                if (spins > SPIN_CAP) { dead = true; break; }
                __builtin_amdgcn_s_sleep(4);
            }
            *(LAS v4u*)(L + L_QF + lane * 16) = v4u{g0.x, g0.z, g1.x, g1.z};
            *(LAS v4u*)(L + L_QF + 1024 + lane * 16) = v4u{g2.x, g2.z, g3.x, g3.z};
            asm volatile("s_waitcnt lgkmcnt(0)" ::: "memory");
            if (lane == 0) flags[0] = dead ? 2u : 1u;
        }
        STAMP(4);
        {
            const GAS v4u* xp = (const GAS v4u*)p.x; const GAS v4u* gp = (const GAS v4u*)p.gw; const GAS v4u* rp = (const GAS v4u*)p.wr;
            float sg = 0.f, sr = 0.f;
#pragma unroll
            for (int i = 0; i < 2; ++i) {
                const v4u xv = xp[lane + 64 * i], gv = gp[lane + 64 * i], rv = rp[lane + 64 * i];
#pragma unroll
                for (int k = 0; k < 4; ++k) {
                    sg += rbf(bflo(xv[k]) * bflo(gv[k])); sg += rbf(bfhi(xv[k]) * bfhi(gv[k]));
                    sr = fmaf(bflo(xv[k]), bflo(rv[k]), sr); sr = fmaf(bfhi(xv[k]), bfhi(rv[k]), sr);
                }
            }
            sg = rbf(wave_sum(sg)); sr = rbf(wave_sum(sr));
            const float g_gate = rbf(1.f / (1.f + __expf(-sg)));
            const float rsg = rbf(1.f / (1.f + __expf(-sr)));
            if (lane == 0) *(LAS f32x2*)(L + L_SC) = f32x2{g_gate, (1.f - g_gate) * rsg};
        }
        lds_wait_slow(flags, 6, 1u);
        {
            const int xo = WS_H + w * H_STRIDE;
            const bool used = (lane & 15) < 14;
            v4u d[8];
            for (unsigned spins = 0;; ++spins) {
                asm volatile("" ::: "memory");
#pragma unroll
                for (int i = 0; i < 8; ++i) d[i] = ld_sc1(rs, xo + i * 1024 + lane * 16);
                bool ok = true;
#pragma unroll
                for (int i = 0; i < 8; ++i) ok = ok && d[i].y == tag && d[i].w == tag;
                if (__all(ok || !used) || dead) break;
                if (spins > SPIN_CAP) { dead = true; break; }
                __builtin_amdgcn_s_sleep(32);
            }
            float s0 = 0.f, s1 = 0.f;
#pragma unroll
            for (int i = 0; i < 8; ++i) { s0 += uf(d[i].x); s1 += uf(d[i].z); }
            s0 = rbf(x16x32_sum(s0)); s1 = rbf(x16x32_sum(s1));
            const float u0 = __shfl(s0, (lane & 15) + 7), u1 = __shfl(s1, (lane & 15) + 7);
            const float a0 = rbf(s0 / (1.f + __expf(-s0))), a1 = rbf(s1 / (1.f + __expf(-s1)));
            float h0 = rbf(a0 * u0), h1 = rbf(a1 * u1);
            if (lane >= 7) { h0 = 0.f; h1 = 0.f; }
            if (dead) h0 = __builtin_nanf("");
            if (lane < 8) *(LAS f32x2*)(L + L_HV + lane * 8) = f32x2{h0, h1};
        }
        STAMP(5);
        WG_BARRIER();
        STAMP_FLUSH();
        return;
    }

    const int grow = 32 * rb + 4 * wv;
    const unsigned char* wq_p = (const unsigned char*)p.wq + (size_t)grow * (NQ * 2) + cb * 1024 + lane * 16;
    const size_t kv_base = ((size_t)kvh * SK + (size_t)sp * 1024 + wv * 4) * 256;
    const unsigned char* k_p = (const unsigned char*)p.kc + kv_base + ((lane >> 2) & 3) * 256 + (4 * (lane >> 4) + (lane & 3)) * 16;
    const unsigned char* v_p = (const unsigned char*)p.vc + kv_base + lane * 16;
    const int glane = lane < 56 ? lane : 55;
    const size_t goff = (size_t)grow * (FF * 2) + cb * 896 + glane * 16;
    const unsigned char* wg_p = (const unsigned char*)p.wg + goff;
    const unsigned char* wu_p = (const unsigned char*)p.wu + goff;
    const unsigned char* wd_p = (const unsigned char*)p.wd + (size_t)(w * 14) * (DM * 2) + wv * 256 + (lane & 15) * 16;
    const unsigned char* wo_p = (const unsigned char*)p.wo + (size_t)(w * 16 + (lane >> 4)) * (DM * 2) + wv * 256 + (lane & 15) * 16;

    auto issue_w = [&](int i) -> v4u {
        if (i < 4) return ldnt(wq_p + (size_t)i * (NQ * 2));
        if (i < 8) return ldnt(wg_p + (size_t)(i - 4) * (FF * 2));
        return ldnt(wu_p + (size_t)(i - 8) * (FF * 2));
    };
    auto issue = [&](int e) -> v4u {
        if (e < E_WD) {
            const int r = e / (2 * TPR), k = e % (2 * TPR);
            return k < TPR ? ldnt(k_p + (size_t)(r * TPR + k) * 8192) : ldnt(v_p + (size_t)(r * TPR + k - TPR) * 8192);
        }
        if (e < E_WO) { const int r = 4 * (e - E_WD) + (lane >> 4); return ldnt(wd_p + (size_t)(r < 14 ? r : 13) * (DM * 2)); }
        return ldnt(wo_p + (size_t)(e - E_WO) * 4 * (DM * 2));
    };
    v4u ring[DT];
#define ISSUE(j) do { THROTTLE(); ring[(j) % DT] = issue(j); SB(); } while (0)
#define AFTER(e) do { if (jmax(e) > ((e) == 0 ? DT - 1 : jmax((e) - 1))) ISSUE(jmax(e)); } while (0)
#define GET(e) ring[(e) % DT]

    const v2u xg = *(const GAS v2u*)(p.x + grow);
    v4u wt[12];
#pragma unroll
    for (int i = 0; i < 4; ++i) wt[i] = issue_w(i);
    SB();
    WG_BARRIER();
#pragma unroll
    for (int i = 4; i < 4 + EARLY; ++i) wt[i] = issue_w(i);
    SB();
    bool dead = false;
    {
        const float xv[4] = {bflo(xg.x), bfhi(xg.x), bflo(xg.y), bfhi(xg.y)};
        float acc[8] = {0.f, 0.f, 0.f, 0.f, 0.f, 0.f, 0.f, 0.f};
#pragma unroll
        for (int t = 0; t < 4; ++t) fma8(acc, wt[t], xv[t]);
        {
            LAS unsigned char* q = L + L_QP + (wv * 512 + lane * 8) * 4;
            *(LAS f32x4*)(q) = f32x4{acc[0], acc[1], acc[2], acc[3]}; *(LAS f32x4*)(q + 16) = f32x4{acc[4], acc[5], acc[6], acc[7]};
        }
        lds_arrive(L, 1, lane);
        STAMP(1);
        float ag[8] = {0.f, 0.f, 0.f, 0.f, 0.f, 0.f, 0.f, 0.f}, au[8] = {0.f, 0.f, 0.f, 0.f, 0.f, 0.f, 0.f, 0.f};
#pragma unroll
        for (int t = 4 + EARLY; t < 12; ++t) { THROTTLE(); wt[t] = issue_w(t); SB(); }
#pragma unroll
        for (int j = 0; j < DEPTH; ++j) ISSUE(j);
#pragma unroll
        for (int t = 0; t < 4; ++t) { fma8(ag, wt[4 + t], xv[t]); }
#pragma unroll
        for (int t = 0; t < 4; ++t) { fma8(au, wt[8 + t], xv[t]); }
        if (lane < 56) {
            LAS unsigned char* q = L + L_GU + (wv * 2 * 448 + lane * 8) * 4;
            *(LAS f32x4*)(q) = f32x4{ag[0], ag[1], ag[2], ag[3]}; *(LAS f32x4*)(q + 16) = f32x4{ag[4], ag[5], ag[6], ag[7]};
            *(LAS f32x4*)(q + 1792) = f32x4{au[0], au[1], au[2], au[3]}; *(LAS f32x4*)(q + 1792 + 16) = f32x4{au[4], au[5], au[6], au[7]};
        }
        lds_arrive(L, 2, lane);
    }
#pragma unroll
    for (int j = DEPTH; j < DT; ++j) {
        ISSUE(j);
        if (j == PROG1 && wv == 0 && lane == 0) flags[4] = 1u;
        if (j == PROG2 && wv == 0 && lane == 0) flags[5] = 1u;
    }
    STAMP(2);
    lds_wait(flags, 0, 1u);
    if (flags[0] != 1u) dead = true;
    const v4u qf0 = *(const LAS v4u*)(L + L_QF + lane * 16), qf1 = *(const LAS v4u*)(L + L_QF + 1024 + lane * 16);
    STAMP(3);

    constexpr float C2 = 0.088388347648318440f * 1.4426950408889634f;
    const unsigned mk0 = (lane & 3) == 0 ? 0x0000ffffu : (lane & 3) == 1 ? 0xffff0000u : 0u;
    const unsigned mk1 = (lane & 3) == 2 ? 0x0000ffffu : (lane & 3) == 3 ? 0xffff0000u : 0u;
    const int sub = lane & 3;
    f32x4 o00 = {0.f, 0.f, 0.f, 0.f}, o01 = o00, o10 = o00, o11 = o00;
    float m0 = 0.f, m1 = 0.f, l0 = 0.f, l1 = 0.f;
#pragma unroll
    for (int r = 0; r < NROUND; ++r) {
        const int base = r * 2 * TPR;
        const f32x4 z = {0.f, 0.f, 0.f, 0.f};
        f32x4 d0[TPR], d1[TPR];
        float P0, P1;
        d0[0] = mfma32(GET(base + 0), qf0, z); d1[0] = mfma32(GET(base + 0), qf1, z); AFTER(base + 0);
        d0[1] = mfma32(GET(base + 1), qf0, z); d1[1] = mfma32(GET(base + 1), qf1, z); AFTER(base + 1);
        d0[2] = mfma32(GET(base + 2), qf0, z); d1[2] = mfma32(GET(base + 2), qf1, z);
        P0 = qred(d0[0]); P1 = qred(d1[0]);
        AFTER(base + 2);
        d0[3] = mfma32(GET(base + 3), qf0, z); d1[3] = mfma32(GET(base + 3), qf1, z);
        { const float a = qred(d0[1]), c = qred(d1[1]); P0 = sub == 1 ? a : P0; P1 = sub == 1 ? c : P1; }
        AFTER(base + 3);
        { const float a = qred(d0[2]), c = qred(d1[2]); P0 = sub == 2 ? a : P0; P1 = sub == 2 ? c : P1; }
        { const float a = qred(d0[3]), c = qred(d1[3]); P0 = sub == 3 ? a : P0; P1 = sub == 3 ? c : P1; }
        float t0 = fmaxf(P0, dpp<XOR1>(P0)), t1 = fmaxf(P1, dpp<XOR1>(P1));
        t0 = fmaxf(t0, dpp<XOR2>(t0)); t1 = fmaxf(t1, dpp<XOR2>(t1));
        t0 = x16x32_max(t0) * C2; t1 = x16x32_max(t1) * C2;
        if (r == 0) { m0 = t0; m1 = t1; }
        else if (__any(t0 > m0 || t1 > m1)) {
            const float n0 = fmaxf(m0, t0), n1 = fmaxf(m1, t1);
            const float a0 = __builtin_amdgcn_exp2f(m0 - n0), a1 = __builtin_amdgcn_exp2f(m1 - n1);
            m0 = n0; m1 = n1; l0 *= a0; l1 *= a1;
            const float b0 = __shfl(a0, (lane >> 4) * 20), b1 = __shfl(a1, (lane >> 4) * 20);
            o00 *= b0; o01 *= b0; o10 *= b1; o11 *= b1;
        }
        const float p0 = __builtin_amdgcn_exp2f(fmaf(P0, C2, -m0)), p1 = __builtin_amdgcn_exp2f(fmaf(P1, C2, -m1));
        l0 += p0; l1 += p1;
        const unsigned pp0 = pk(p0, p0), pp1 = pk(p1, p1);
        if (PRIO && r == 0 && wv >= 4) __builtin_amdgcn_s_setprio(1);
#define PV(t) do { \
            const unsigned b0_ = qbc<t>(pp0), b1_ = qbc<t>(pp1); \
            const v2u a0_ = {b0_ & mk0, b0_ & mk1}, a1_ = {b1_ & mk0, b1_ & mk1}; \
            const v4u vf_ = GET(base + TPR + (t)); \
            o00 = mfma16(a0_, v2u{vf_.x, vf_.y}, o00); o01 = mfma16(a0_, v2u{vf_.z, vf_.w}, o01); \
            o10 = mfma16(a1_, v2u{vf_.x, vf_.y}, o10); o11 = mfma16(a1_, v2u{vf_.z, vf_.w}, o11); \
            AFTER(base + TPR + (t)); } while (0)
        PV(0); PV(1); PV(2); PV(3);
        if (r == NROUND / 2 - 1) { STAMP(4); if (wv == 0 && lane == 0) flags[6] = 1u; }
    }
#pragma unroll
    for (int j = jmax(E_WD - 1) + 1; j < E_END; ++j) ring[j % DT] = issue(j);
    SB();
    STAMP(5);
    l0 += dpp<XOR1>(l0); l1 += dpp<XOR1>(l1); l0 += dpp<XOR2>(l0); l1 += dpp<XOR2>(l1);
    l0 = x16x32_sum(l0); l1 = x16x32_sum(l1);
    {
        LAS unsigned char* mb = L + L_MRG + wv * 4096 + (lane >> 4) * 512 + (lane & 15) * 32;
        *(LAS f32x4*)(mb) = o00; *(LAS f32x4*)(mb + 16) = o01;
        *(LAS f32x4*)(mb + 2048) = o10; *(LAS f32x4*)(mb + 2048 + 16) = o11;
        if ((lane & 0x33) == 0) {
            LAS unsigned char* q = L + L_ML + wv * 64 + (lane >> 2) * 8;
            *(LAS f32x2*)(q) = f32x2{m0, dead ? __builtin_nanf("") : l0}; *(LAS f32x2*)(q + 32) = f32x2{m1, l1};
        }
    }
    WG_BARRIER();
    STAMP(6);
    const unsigned tag = flags[3];
    {
        f32x2 ml[NSW];
#pragma unroll
        for (int i = 0; i < NSW; ++i) ml[i] = *(const LAS f32x2*)(L + L_ML + i * 64 + wv * 8);
        float M = ml[0][0];
#pragma unroll
        for (int i = 1; i < NSW; ++i) M = fmaxf(M, ml[i][0]);
        float Ls = 0.f, a0 = 0.f, a1 = 0.f;
#pragma unroll
        for (int i = 0; i < NSW; ++i) {
            const float wt_ = __builtin_amdgcn_exp2f(ml[i][0] - M);
            const f32x2 o = *(const LAS f32x2*)(L + L_MRG + i * 4096 + wv * 512 + lane * 8);
            Ls = fmaf(wt_, ml[i][1], Ls); a0 = fmaf(wt_, o[0], a0); a1 = fmaf(wt_, o[1], a1);
        }
        st_sc1(rs, WS_X1 + (kvh * 64 + wv * 8 + (lane >> 3)) * X1_STRIDE + sp * 128 + (lane & 7) * 16, v4u{fu(a0), tag, fu(a1), tag});
        if (lane == 0) st_sc1(rs, WS_ML + ((kvh * 8 + wv) * 64 + sp) * 16, v4u{fu(M), tag, fu(Ls), tag});
    }
    STAMP(7);
    {
        const int xo = WS_X1 + w * X1_STRIDE + wv * 1024 + lane * 16;
        const int mo = WS_ML + ((w >> 3) * 64 + wv * 8 + (lane >> 3)) * 16;
        v4u d, ml;
        unsigned nsp = 0;
        for (unsigned spins = 0;; ++spins) {
            asm volatile("" ::: "memory");
            ml = ld_sc1(rs, mo);
            ++nsp;
            if (__all(ml.y == tag && ml.w == tag)) break;
            if (spins > SPIN_CAP) { dead = true; break; }
        }
        STAMP(10);
        for (unsigned spins = 0;; ++spins) {
            asm volatile("" ::: "memory");
            d = ld_sc1(rs, xo);
            if (__all(d.y == tag && d.w == tag) || dead) break;
            if (spins > SPIN_CAP) { dead = true; break; }
        }
        const float m = uf(ml.x);
        float M = fmaxf(m, dpp<XOR8>(m)); M = x16x32_max(M);
        const float wt_ = __builtin_amdgcn_exp2f(m - M);
        float Ls = wt_ * uf(ml.z), o0 = wt_ * uf(d.x), o1 = wt_ * uf(d.z);
        Ls += dpp<XOR8>(Ls); o0 += dpp<XOR8>(o0); o1 += dpp<XOR8>(o1);
        Ls = x16x32_sum(Ls); o0 = x16x32_sum(o0); o1 = x16x32_sum(o1);
        if (lane < 8) {
            *(LAS float*)(L + L_X1 + 64 + ((2 * lane) * 8 + wv) * 4) = o0;
            *(LAS float*)(L + L_X1 + 64 + ((2 * lane + 1) * 8 + wv) * 4) = o1;
            if (lane == 0) *(LAS f32x2*)(L + L_X1 + wv * 8) = f32x2{M, dead ? __builtin_nanf("") : Ls};
        }
    }
    STAMP(8);
    WG_BARRIER();
    const f32x2 sc = *(const LAS f32x2*)(L + L_SC);
    {
        f32x2 ml[NSW];
#pragma unroll
        for (int i = 0; i < NSW; ++i) ml[i] = *(const LAS f32x2*)(L + L_X1 + i * 8);
        float M = ml[0][0];
#pragma unroll
        for (int i = 1; i < NSW; ++i) M = fmaxf(M, ml[i][0]);
        float wt_[NSW], Ls = 0.f;
#pragma unroll
        for (int i = 0; i < NSW; ++i) { wt_[i] = __builtin_amdgcn_exp2f(ml[i][0] - M); Ls = fmaf(wt_[i], ml[i][1], Ls); }
        const float inv = 1.f / Ls;
        f32x4 acc[2] = {{0.f, 0.f, 0.f, 0.f}, {0.f, 0.f, 0.f, 0.f}};
#pragma unroll
        for (int t = 0; t < 4; ++t) {
            const float h = *(const LAS float*)(L + L_HV + (4 * t + (lane >> 4)) * 4) * sc[1];
            rows_mfma(acc, GET(E_WD + t), pk(h, h), mk0, mk1);
        }
#pragma unroll
        for (int t = 0; t < 4; ++t) {
            const LAS unsigned char* q = L + L_X1 + 64 + (4 * t + (lane >> 4)) * 32;
            const f32x4 u = *(const LAS f32x4*)(q), v = *(const LAS f32x4*)(q + 16);
            float o = wt_[0] * u[0];
            o = fmaf(wt_[1], u[1], o); o = fmaf(wt_[2], u[2], o); o = fmaf(wt_[3], u[3], o);
            o = fmaf(wt_[4], v[0], o); o = fmaf(wt_[5], v[1], o); o = fmaf(wt_[6], v[2], o); o = fmaf(wt_[7], v[3], o);
            o = rbf(o * inv) * sc[0];
            rows_mfma(acc, GET(E_WO + t), pk(o, o), mk0, mk1);
        }
        const int g2 = lane >> 4, c = lane & 15;
        const f32x4 a = (g2 & 2) ? acc[1] : acc[0];
        const float v0 = (g2 & 1) ? a[2] : a[0], v1 = (g2 & 1) ? a[3] : a[1];
        const int cons = 8 * wv + (c >> 1);
        st_sc1(rs, WS_X2 + cons * X2_STRIDE + w * 128 + ((c & 1) * 4 + g2) * 16, v4u{fu(v0), tag, fu(v1), tag});
    }
    STAMP(9);
    if ((w & 3) == 0) {
        const int xo = WS_X2 + (w >> 2) * X2_STRIDE + wv * 4096 + lane * 16;
        const unsigned xv = *(const GAS unsigned*)(p.x + 16 * (w >> 2) + 2 * (lane & 7));
        v4u d[4];
        unsigned done = 0u;
        for (unsigned spins = 0;; ++spins) {
            asm volatile("" ::: "memory");
#pragma unroll
            for (int i = 0; i < 4; ++i) if (!((done >> i) & 1u)) d[i] = ld_sc1(rs, xo + i * 1024);
#pragma unroll
            for (int i = 0; i < 4; ++i) if (__all(d[i].y == tag && d[i].w == tag)) done |= 1u << i;
            if (done == 0xfu) break;
            if (spins > SPIN_CAP) { dead = true; break; }
        }
        float s0 = (uf(d[0].x) + uf(d[1].x)) + (uf(d[2].x) + uf(d[3].x)), s1 = (uf(d[0].z) + uf(d[1].z)) + (uf(d[2].z) + uf(d[3].z));
        s0 += dpp<XOR8>(s0); s1 += dpp<XOR8>(s1);
        s0 = x16x32_sum(s0); s1 = x16x32_sum(s1);
        if (dead) s0 = __builtin_nanf("");
        if (lane < 8) *(LAS f32x2*)(L + L_X2 + wv * 64 + lane * 8) = f32x2{s0, s1};
        WG_BARRIER();
        if (wv == 0 && lane < 8) {
            f32x2 s = {0.f, 0.f};
#pragma unroll
            for (int i = 0; i < NSW; ++i) s += *(const LAS f32x2*)(L + L_X2 + i * 64 + lane * 8);
            const int col = 16 * (w >> 2) + 2 * lane;
            *(GAS unsigned*)(p.out + col) = pk(fmaf(1.f - sc[0], bflo(xv), s[0]), fmaf(1.f - sc[0], bfhi(xv), s[1]));
            if (w == 0 && lane == 0) __builtin_amdgcn_raw_buffer_store_b32(tag, rs, WS_EPOCH, 0, AUX_SC1);
        }
        STAMP(11);
    }
    STAMP_FLUSH();
}

extern "C" void kernel_launch(void* const* d_in, const int* in_sizes, int n_in, void* d_out, int out_size, void* d_ws, size_t ws_size,
                              hipStream_t stream) {
    Params p;
    p.x = (const unsigned short*)d_in[0];
    p.kc = (const unsigned short*)d_in[1];
    p.vc = (const unsigned short*)d_in[2];
    p.wq = (const unsigned short*)d_in[3];
    p.wo = (const unsigned short*)d_in[4];
    p.wr = (const unsigned short*)d_in[6];
    p.wg = (const unsigned short*)d_in[7];
    p.wu = (const unsigned short*)d_in[8];
    p.wd = (const unsigned short*)d_in[9];
    p.gw = (const unsigned short*)d_in[10];
    p.out = (unsigned short*)d_out;
    p.ws = (unsigned char*)d_ws;
    (void)hipFuncSetAttribute((const void*)fused_decode_kernel, hipFuncAttributeMaxDynamicSharedMemorySize, LDS_BYTES);
    hipLaunchKernelGGL(fused_decode_kernel, dim3(NWG), dim3(NTHR), LDS_BYTES, stream, p);
}
```

```cpp
#include <hip/hip_runtime.h>
#include <hip/hip_bf16.h>
#include <stdint.h>

#define GAS __attribute__((address_space(1)))
#define LAS __attribute__((address_space(3)))

typedef unsigned v4u __attribute__((ext_vector_type(4)));
typedef unsigned v2u __attribute__((ext_vector_type(2)));
typedef float f32x4 __attribute__((ext_vector_type(4)));
typedef float f32x2 __attribute__((ext_vector_type(2)));
typedef short bf16x8 __attribute__((ext_vector_type(8)));
typedef short bf16x4 __attribute__((ext_vector_type(4)));
typedef __bf16 bf2 __attribute__((ext_vector_type(2)));

namespace {

constexpr int NWG = 256, NSW = 8, NTHR = (NSW + 1) * 64;
constexpr int DM = 1024, SK = 65536, DH = 128, FF = 3584, NQ = 4096;
#define EARLY 2
#define TAILD 4
#define TAILDM1 3
#define PRIO 0
#define DEPTH 4
#define DTW 28
#define PROG1 4
#define PROG2 13
constexpr int TPR = 4, NROUND = 8;
constexpr int E_END = 64;
constexpr int DT = DTW;
constexpr int jmax(int e) {
    const int a = DT - 1 + (e + 2) / 2, b = e + DEPTH + 1;
    const int m = a > b ? a : b;
    return m < E_END - 1 ? m : E_END - 1;
}

constexpr int WS_EPOCH = 0;
constexpr int WS_Q = 4096;
constexpr int WS_Q1 = WS_Q + 2048 * 8;
constexpr int Q1_STRIDE = 32 * 128;
constexpr int WS_ML = WS_Q1 + NWG * Q1_STRIDE;
constexpr int WS_X1 = WS_ML + 32 * 64 * 16;
constexpr int X1_STRIDE = 64 * 128;
constexpr int WS_X2 = WS_X1 + NWG * X1_STRIDE;
constexpr int X2_STRIDE = NWG * 128;
constexpr int WS_H = WS_X2 + 64 * X2_STRIDE;
constexpr int H_STRIDE = 32 * 256;
constexpr int WS_END = WS_H + NWG * H_STRIDE;

constexpr int L_FLAGS = 0;
constexpr int L_HV = 128;
constexpr int L_SC = 192;
constexpr int L_QF = 256;
constexpr int L_ML = L_QF + 2048;
constexpr int L_X1 = L_ML + 512;
constexpr int L_X2 = L_X1 + 64 + 512;
constexpr int L_GU = L_X2 + 512;
constexpr int L_QP = L_GU + 8 * 2 * 448 * 4;
constexpr int L_MRG = L_GU;
constexpr int L_END = L_QP + 8 * 512 * 4;
constexpr int LDS_BYTES = L_END;
static_assert(8 * 8 * 128 * 4 <= L_END - L_GU, "alias");
static_assert(L_MRG % 16 == 0 && L_GU % 16 == 0 && L_QP % 16 == 0, "align");

constexpr unsigned SPIN_CAP = 1u << 18;
constexpr int AUX_SC1 = 16;

struct Params {
    const unsigned short* x;
    const unsigned short* kc;
    const unsigned short* vc;
    const unsigned short* wq;
    const unsigned short* wo;
    const unsigned short* wr;
    const unsigned short* wg;
    const unsigned short* wu;
    const unsigned short* wd;
    const unsigned short* gw;
    unsigned short* out;
    unsigned char* ws;
};

__device__ __forceinline__ v4u ldnt(const unsigned char* p) { return __builtin_nontemporal_load((const GAS v4u*)p); }
__device__ __forceinline__ float bflo(unsigned u) { return __builtin_bit_cast(float, u << 16); }
__device__ __forceinline__ float bfhi(unsigned u) { return __builtin_bit_cast(float, u & 0xffff0000u); }
__device__ __forceinline__ unsigned pk(float lo, float hi) {
    bf2 r = {(__bf16)lo, (__bf16)hi};
    return __builtin_bit_cast(unsigned, r);
}
__device__ __forceinline__ float rbf(float v) { return bflo(pk(v, v)); }
__device__ __forceinline__ unsigned fu(float f) { return __builtin_bit_cast(unsigned, f); }
__device__ __forceinline__ float uf(unsigned u) { return __builtin_bit_cast(float, u); }
template <int CTRL> __device__ __forceinline__ float dpp(float x) {
    return uf((unsigned)__builtin_amdgcn_mov_dpp((int)fu(x), CTRL, 0xf, 0xf, true));
}
constexpr int XOR1 = 0xB1, XOR2 = 0x4E, XOR7 = 0x141, XOR8 = 0x128, ROR4 = 0x124;
__device__ __forceinline__ float swap32_add(float a, float b) {
    auto s = __builtin_amdgcn_permlane32_swap(fu(a), fu(b), false, false);
    return uf(s[0]) + uf(s[1]);
}
__device__ __forceinline__ float swap16_add(float a, float b) {
    auto s = __builtin_amdgcn_permlane16_swap(fu(a), fu(b), false, false);
    return uf(s[0]) + uf(s[1]);
}
__device__ __forceinline__ float x16x32_max(float x) {
    auto s = __builtin_amdgcn_permlane16_swap(fu(x), fu(x), false, false);
    x = fmaxf(uf(s[0]), uf(s[1]));
    auto t = __builtin_amdgcn_permlane32_swap(fu(x), fu(x), false, false);
    return fmaxf(uf(t[0]), uf(t[1]));
}
__device__ __forceinline__ float x16x32_sum(float x) { x = swap16_add(x, x); return swap32_add(x, x); }
__device__ __forceinline__ float row_sum(float x) {
    x += dpp<XOR1>(x); x += dpp<XOR2>(x); x += dpp<XOR7>(x); x += dpp<XOR8>(x); return x;
}
__device__ __forceinline__ float row_max(float x) {
    x = fmaxf(x, dpp<XOR1>(x)); x = fmaxf(x, dpp<XOR2>(x)); x = fmaxf(x, dpp<XOR7>(x)); x = fmaxf(x, dpp<XOR8>(x)); return x;
}
__device__ __forceinline__ float wave_sum(float x) { return x16x32_sum(row_sum(x)); }
__device__ __forceinline__ float wave_max(float x) { return x16x32_max(row_max(x)); }

__device__ __forceinline__ f32x4 mfma32(v4u a, v4u b, f32x4 c) {
    return __builtin_amdgcn_mfma_f32_16x16x32_bf16(__builtin_bit_cast(bf16x8, a), __builtin_bit_cast(bf16x8, b), c, 0, 0, 0);
}
__device__ __forceinline__ f32x4 mfma16(v2u a, v2u b, f32x4 c) {
    return __builtin_amdgcn_mfma_f32_16x16x16bf16_1k(__builtin_bit_cast(bf16x4, a), __builtin_bit_cast(bf16x4, b), c, 0, 0, 0);
}
#define WG_BARRIER() do { asm volatile("s_waitcnt lgkmcnt(0)" ::: "memory"); __builtin_amdgcn_s_barrier(); asm volatile("" ::: "memory"); } while (0)
#define SB() __builtin_amdgcn_sched_barrier(0)
#define SBMASK 0x10F
#define SBM() __builtin_amdgcn_sched_barrier(SBMASK)
#define STR2(x) #x
#define STR(x) STR2(x)
#define THROTTLE() asm volatile("s_waitcnt vmcnt(" STR(DEPTHM1) ")" ::: "memory")
#define DEPTHM1 3

__device__ __forceinline__ void fma8(float (&acc)[8], v4u wv, float xv) {
    acc[0] = fmaf(xv, bflo(wv.x), acc[0]); acc[1] = fmaf(xv, bfhi(wv.x), acc[1]);
    acc[2] = fmaf(xv, bflo(wv.y), acc[2]); acc[3] = fmaf(xv, bfhi(wv.y), acc[3]);
    acc[4] = fmaf(xv, bflo(wv.z), acc[4]); acc[5] = fmaf(xv, bfhi(wv.z), acc[5]);
    acc[6] = fmaf(xv, bflo(wv.w), acc[6]); acc[7] = fmaf(xv, bfhi(wv.w), acc[7]);
}
__device__ __forceinline__ f32x2 reduce_cols(const float (&a)[8]) {
    const float b0 = swap32_add(a[0], a[4]), b1 = swap32_add(a[1], a[5]), b2 = swap32_add(a[2], a[6]), b3 = swap32_add(a[3], a[7]);
    float c0 = swap16_add(b0, b2), c1 = swap16_add(b1, b3);
    c0 += dpp<XOR2>(c0); c1 += dpp<XOR2>(c1);
    c0 += dpp<ROR4>(c0); c1 += dpp<ROR4>(c1);
    c0 += dpp<XOR8>(c0); c1 += dpp<XOR8>(c1);
    return f32x2{c0, c1};
}

__device__ __forceinline__ v4u ld_sc1(__amdgpu_buffer_rsrc_t rs, int off) { return __builtin_amdgcn_raw_buffer_load_b128(rs, off, 0, AUX_SC1); }
__device__ __forceinline__ void st_sc1(__amdgpu_buffer_rsrc_t rs, int off, v4u v) { __builtin_amdgcn_raw_buffer_store_b128(v, rs, off, 0, AUX_SC1); }

__device__ __forceinline__ void rows_mfma(f32x4 (&acc)[2], v4u wv, unsigned pp, unsigned m0, unsigned m1) {
    const v2u a = {pp & m0, pp & m1};
    acc[0] = mfma16(a, v2u{wv.x, wv.y}, acc[0]);
    acc[1] = mfma16(a, v2u{wv.z, wv.w}, acc[1]);
}
__device__ __forceinline__ unsigned lds_arrive(LAS unsigned char* L, int word, int lane) {
    asm volatile("s_waitcnt lgkmcnt(0)" ::: "memory");
    unsigned old = 0;
    if (lane == 0) old = __hip_atomic_fetch_add((LAS unsigned*)(L + L_FLAGS + 4 * word), 1u, __ATOMIC_RELAXED, __HIP_MEMORY_SCOPE_WORKGROUP);
    old = __builtin_amdgcn_readfirstlane(old);
    asm volatile("" ::: "memory");
    return old;
}
__device__ __forceinline__ void lds_wait_slow(volatile LAS unsigned* flags, int word, unsigned want) {
    for (unsigned spins = 0; flags[word] < want && spins < SPIN_CAP; ++spins) __builtin_amdgcn_s_sleep(8);
    asm volatile("" ::: "memory");
}
__device__ __forceinline__ void lds_wait(volatile LAS unsigned* flags, int word, unsigned want) {
    for (unsigned spins = 0; flags[word] < want && spins < (SPIN_CAP << 4); ++spins) __builtin_amdgcn_s_sleep(1);
    asm volatile("" ::: "memory");
}
__device__ __forceinline__ float qred(f32x4 d) {
    float t = dpp<0x00>(d[0]);
    t = dpp<0x55>(d[1]) + t;
    asm volatile("" : "+v"(t));
    t = dpp<0xAA>(d[2]) + t;
    t = dpp<0xFF>(d[3]) + t;
    return t;
}
template <int T> __device__ __forceinline__ unsigned qbc(unsigned x) {
    return (unsigned)__builtin_amdgcn_mov_dpp((int)x, T * 0x55, 0xf, 0xf, true);
}

#define STAMP(i) do {} while (0)
#define STAMP_DECL() do {} while (0)
#define STAMP_FLUSH() do {} while (0)

}

__global__ void __launch_bounds__(NTHR, 1) fused_decode_kernel(Params p) {
    extern __shared__ __attribute__((aligned(16))) unsigned char smem[];
    LAS unsigned char* L = (LAS unsigned char*)smem;
    volatile LAS unsigned* flags = (volatile LAS unsigned*)(L + L_FLAGS);
    const int tid = threadIdx.x, lane = tid & 63;
    const int wv = __builtin_amdgcn_readfirstlane(tid >> 6);
    const int b = blockIdx.x;
    const int w = (b & 7) * 32 + (b >> 3);
    const int kvh = w >> 6, sp = w & 63;
    const int cb = w & 7, rb = w >> 3;
    const __amdgpu_buffer_rsrc_t rs = __builtin_amdgcn_make_buffer_rsrc((void*)p.ws, 0, WS_END, 0x00020000);
    STAMP_DECL();
    STAMP(0);

    if (wv == NSW) {
        const unsigned ep = __builtin_amdgcn_raw_buffer_load_b32(rs, WS_EPOCH, 0, AUX_SC1);
        if (lane < 8) flags[lane] = 0u;
        WG_BARRIER();
        const unsigned tag = __builtin_amdgcn_readfirstlane(ep) + 1u;
        if (lane == 0) flags[3] = tag;
        bool dead = false;
        STAMP(1);
        lds_wait(flags, 1, NSW);
#pragma unroll
        for (int it = 0; it < 4; ++it) {
            const int c = it * 128 + lane * 2;
            f32x2 s = {0.f, 0.f};
#pragma unroll
            for (int i = 0; i < NSW; ++i) s += *(const LAS f32x2*)(L + L_QP + (i * 512 + c) * 4);
            st_sc1(rs, WS_Q1 + (cb * 32 + (c >> 4)) * Q1_STRIDE + rb * 128 + (c & 15) * 8, v4u{fu(s[0]), tag, fu(s[1]), tag});
        }
        STAMP(2);
        lds_wait_slow(flags, 4, 1u);
        {
            const int xo = WS_Q1 + w * Q1_STRIDE;
            v4u d[4];
            for (unsigned spins = 0;; ++spins) {
                asm volatile("" ::: "memory");
#pragma unroll
                for (int i = 0; i < 4; ++i) d[i] = ld_sc1(rs, xo + i * 1024 + lane * 16);
                bool ok = true;
#pragma unroll
                for (int i = 0; i < 4; ++i) ok = ok && d[i].y == tag && d[i].w == tag;
                if (__all(ok)) break;
                if (spins > SPIN_CAP) { dead = true; break; }
                __builtin_amdgcn_s_sleep(8);
            }
            float s0 = (uf(d[0].x) + uf(d[1].x)) + (uf(d[2].x) + uf(d[3].x)), s1 = (uf(d[0].z) + uf(d[1].z)) + (uf(d[2].z) + uf(d[3].z));
            s0 += dpp<XOR8>(s0); s1 += dpp<XOR8>(s1);
            s0 = x16x32_sum(s0); s1 = x16x32_sum(s1);
            const unsigned pr = pk(s0, s1);
            const unsigned nx = __builtin_amdgcn_mov_dpp((int)pr, XOR1, 0xf, 0xf, true);
            if (lane < 8 && !(lane & 1) && !dead) st_sc1(rs, WS_Q + (w * 8 + lane) * 8, v4u{pr, tag, nx, tag});
        }
        STAMP(3);
        lds_wait(flags, 2, NSW);
#pragma unroll
        for (int it = 0; it < 7; ++it) {
            const int idx = it * 64 + lane, j = idx / 14, k = idx - 14 * j;
            const int mat = k >= 7 ? 1 : 0, u = 14 * j + 2 * (k - 7 * mat);
            f32x2 s = {0.f, 0.f};
#pragma unroll
            for (int i = 0; i < NSW; ++i) s += *(const LAS f32x2*)(L + L_GU + ((i * 2 + mat) * 448 + u) * 4);
            st_sc1(rs, WS_H + (cb * 32 + j) * H_STRIDE + rb * 256 + k * 16, v4u{fu(s[0]), tag, fu(s[1]), tag});
        }
        lds_wait_slow(flags, 5, 1u);
        {
            const int qoff = WS_Q + (kvh * 512 + ((lane >> 2) & 3) * 64 + (4 * (lane >> 4) + (lane & 3)) * 4) * 8;
            v4u g0, g1, g2, g3;
            for (unsigned spins = 0;; ++spins) {
                asm volatile("" ::: "memory");
                g0 = ld_sc1(rs, qoff); g1 = ld_sc1(rs, qoff + 16); g2 = ld_sc1(rs, qoff + 2048); g3 = ld_sc1(rs, qoff + 2048 + 16);
                const bool ok = g0.y == tag && g0.w == tag && g1.y == tag && g1.w == tag && g2.y == tag && g2.w == tag && g3.y == tag && g3.w == tag;
                if (__all(ok) || dead) break;
                if (spins > SPIN_CAP) { dead = true; break; }
                __builtin_amdgcn_s_sleep(4);
            }
            *(LAS v4u*)(L + L_QF + lane * 16) = v4u{g0.x, g0.z, g1.x, g1.z};
            *(LAS v4u*)(L + L_QF + 1024 + lane * 16) = v4u{g2.x, g2.z, g3.x, g3.z};
            asm volatile("s_waitcnt lgkmcnt(0)" ::: "memory");
            if (lane == 0) flags[0] = dead ? 2u : 1u;
        }
        STAMP(4);
        {
            const GAS v4u* xp = (const GAS v4u*)p.x; const GAS v4u* gp = (const GAS v4u*)p.gw; const GAS v4u* rp = (const GAS v4u*)p.wr;
            float sg = 0.f, sr = 0.f;
#pragma unroll
            for (int i = 0; i < 2; ++i) {
                const v4u xv = xp[lane + 64 * i], gv = gp[lane + 64 * i], rv = rp[lane + 64 * i];
#pragma unroll
                for (int k = 0; k < 4; ++k) {
                    sg += rbf(bflo(xv[k]) * bflo(gv[k])); sg += rbf(bfhi(xv[k]) * bfhi(gv[k]));
                    sr = fmaf(bflo(xv[k]), bflo(rv[k]), sr); sr = fmaf(bfhi(xv[k]), bfhi(rv[k]), sr);
                }
            }
            sg = rbf(wave_sum(sg)); sr = rbf(wave_sum(sr));
            const float g_gate = rbf(1.f / (1.f + __expf(-sg)));
            const float rsg = rbf(1.f / (1.f + __expf(-sr)));
            if (lane == 0) *(LAS f32x2*)(L + L_SC) = f32x2{g_gate, (1.f - g_gate) * rsg};
        }
        lds_wait_slow(flags, 6, 1u);
        {
            const int xo = WS_H + w * H_STRIDE;
            const bool used = (lane & 15) < 14;
            v4u d[8];
            for (unsigned spins = 0;; ++spins) {
                asm volatile("" ::: "memory");
#pragma unroll
                for (int i = 0; i < 8; ++i) d[i] = ld_sc1(rs, xo + i * 1024 + lane * 16);
                bool ok = true;
#pragma unroll
                for (int i = 0; i < 8; ++i) ok = ok && d[i].y == tag && d[i].w == tag;
                if (__all(ok || !used) || dead) break;
                if (spins > SPIN_CAP) { dead = true; break; }
                __builtin_amdgcn_s_sleep(32);
            }
            float s0 = 0.f, s1 = 0.f;
#pragma unroll
            for (int i = 0; i < 8; ++i) { s0 += uf(d[i].x); s1 += uf(d[i].z); }
            s0 = rbf(x16x32_sum(s0)); s1 = rbf(x16x32_sum(s1));
            const float u0 = __shfl(s0, (lane & 15) + 7), u1 = __shfl(s1, (lane & 15) + 7);
            const float a0 = rbf(s0 / (1.f + __expf(-s0))), a1 = rbf(s1 / (1.f + __expf(-s1)));
            float h0 = rbf(a0 * u0), h1 = rbf(a1 * u1);
            if (lane >= 7) { h0 = 0.f; h1 = 0.f; }
            if (dead) h0 = __builtin_nanf("");
            if (lane < 8) *(LAS f32x2*)(L + L_HV + lane * 8) = f32x2{h0, h1};
        }
        STAMP(5);
        WG_BARRIER();
        STAMP_FLUSH();
        return;
    }

    const int grow = 32 * rb + 4 * wv;
    const unsigned char* wq_p = (const unsigned char*)p.wq + (size_t)grow * (NQ * 2) + cb * 1024 + lane * 16;
    const size_t kv_base = ((size_t)kvh * SK + (size_t)sp * 1024 + wv * 4) * 256;
    const unsigned char* k_p = (const unsigned char*)p.kc + kv_base + ((lane >> 2) & 3) * 256 + (4 * (lane >> 4) + (lane & 3)) * 16;
    const unsigned char* v_p = (const unsigned char*)p.vc + kv_base + lane * 16;
    const int glane = lane < 56 ? lane : 55;
    const size_t goff = (size_t)grow * (FF * 2) + cb * 896 + glane * 16;
    const unsigned char* wg_p = (const unsigned char*)p.wg + goff;
    const unsigned char* wu_p = (const unsigned char*)p.wu + goff;
    const unsigned char* wd_p = (const unsigned char*)p.wd + (size_t)(w * 14) * (DM * 2) + wv * 256 + (lane & 15) * 16;
    const unsigned char* wo_p = (const unsigned char*)p.wo + (size_t)(w * 16 + (lane >> 4)) * (DM * 2) + wv * 256 + (lane & 15) * 16;

    auto issue_w = [&](int i) -> v4u {
        if (i < 4) return ldnt(wq_p + (size_t)i * (NQ * 2));
        if (i < 8) return ldnt(wg_p + (size_t)(i - 4) * (FF * 2));
        if (i < 12) return ldnt(wu_p + (size_t)(i - 8) * (FF * 2));
        if (i < 16) { const int r = 4 * (i - 12) + (lane >> 4); return ldnt(wd_p + (size_t)(r < 14 ? r : 13) * (DM * 2)); }
        return ldnt(wo_p + (size_t)(i - 16) * 4 * (DM * 2));
    };
    auto issue = [&](int e) -> v4u {
        const int r = e / (2 * TPR), k = e % (2 * TPR);
        return k < TPR ? ldnt(k_p + (size_t)(r * TPR + k) * 8192) : ldnt(v_p + (size_t)(r * TPR + k - TPR) * 8192);
    };
    v4u ring[DT];
#define ISSUE(j) do { THROTTLE(); ring[(j) % DT] = issue(j); SB(); } while (0)
#define AFTER(e) do { if (jmax(e) > ((e) == 0 ? DT - 1 : jmax((e) - 1))) ISSUE(jmax(e)); } while (0)
#define GET(e) ring[(e) % DT]

    const v2u xg = *(const GAS v2u*)(p.x + grow);
    v4u wt[12];
#pragma unroll
    for (int i = 0; i < 4; ++i) wt[i] = issue_w(i);
    SB();
    WG_BARRIER();
#pragma unroll
    for (int i = 4; i < 4 + EARLY; ++i) wt[i] = issue_w(i);
    SB();
    bool dead = false;
    {
        const float xv[4] = {bflo(xg.x), bfhi(xg.x), bflo(xg.y), bfhi(xg.y)};
        float acc[8] = {0.f, 0.f, 0.f, 0.f, 0.f, 0.f, 0.f, 0.f};
#pragma unroll
        for (int t = 0; t < 4; ++t) fma8(acc, wt[t], xv[t]);
        {
            LAS unsigned char* q = L + L_QP + (wv * 512 + lane * 8) * 4;
            *(LAS f32x4*)(q) = f32x4{acc[0], acc[1], acc[2], acc[3]}; *(LAS f32x4*)(q + 16) = f32x4{acc[4], acc[5], acc[6], acc[7]};
        }
        lds_arrive(L, 1, lane);
        STAMP(1);
        float ag[8] = {0.f, 0.f, 0.f, 0.f, 0.f, 0.f, 0.f, 0.f}, au[8] = {0.f, 0.f, 0.f, 0.f, 0.f, 0.f, 0.f, 0.f};
#pragma unroll
        for (int t = 4 + EARLY; t < 12; ++t) { THROTTLE(); wt[t] = issue_w(t); SB(); }
#pragma unroll
        for (int j = 0; j < 4; ++j) ISSUE(j);
#pragma unroll
        for (int t = 0; t < 4; ++t) { fma8(ag, wt[4 + t], xv[t]); }
#pragma unroll
        for (int t = 0; t < 4; ++t) { fma8(au, wt[8 + t], xv[t]); }
        if (lane < 56) {
            LAS unsigned char* q = L + L_GU + (wv * 2 * 448 + lane * 8) * 4;
            *(LAS f32x4*)(q) = f32x4{ag[0], ag[1], ag[2], ag[3]}; *(LAS f32x4*)(q + 16) = f32x4{ag[4], ag[5], ag[6], ag[7]};
            *(LAS f32x4*)(q + 1792) = f32x4{au[0], au[1], au[2], au[3]}; *(LAS f32x4*)(q + 1792 + 16) = f32x4{au[4], au[5], au[6], au[7]};
        }
        lds_arrive(L, 2, lane);
    }
#pragma unroll
    for (int j = 4; j < DT; ++j) {
        ISSUE(j);
        if (j == PROG1 && wv == 0 && lane == 0) flags[4] = 1u;
        if (j == PROG2 && wv == 0 && lane == 0) flags[5] = 1u;
    }
    STAMP(2);
    lds_wait(flags, 0, 1u);
    if (flags[0] != 1u) dead = true;
    const v4u qf0 = *(const LAS v4u*)(L + L_QF + lane * 16), qf1 = *(const LAS v4u*)(L + L_QF + 1024 + lane * 16);
    STAMP(3);

    constexpr float C2 = 0.088388347648318440f * 1.4426950408889634f;
    const unsigned mk0 = (lane & 3) == 0 ? 0x0000ffffu : (lane & 3) == 1 ? 0xffff0000u : 0u;
    const unsigned mk1 = (lane & 3) == 2 ? 0x0000ffffu : (lane & 3) == 3 ? 0xffff0000u : 0u;
    const int sub = lane & 3;
    f32x4 o00 = {0.f, 0.f, 0.f, 0.f}, o01 = o00, o10 = o00, o11 = o00;
    float m0 = 0.f, m1 = 0.f, l0 = 0.f, l1 = 0.f;
#pragma unroll
    for (int r = 0; r < NROUND; ++r) {
        const int base = r * 2 * TPR;
        const f32x4 z = {0.f, 0.f, 0.f, 0.f};
        f32x4 d0[TPR], d1[TPR];
        float P0, P1;
        d0[0] = mfma32(GET(base + 0), qf0, z); d1[0] = mfma32(GET(base + 0), qf1, z); AFTER(base + 0);
        d0[1] = mfma32(GET(base + 1), qf0, z); d1[1] = mfma32(GET(base + 1), qf1, z); AFTER(base + 1);
        d0[2] = mfma32(GET(base + 2), qf0, z); d1[2] = mfma32(GET(base + 2), qf1, z);
        P0 = qred(d0[0]); P1 = qred(d1[0]);
        AFTER(base + 2);
        d0[3] = mfma32(GET(base + 3), qf0, z); d1[3] = mfma32(GET(base + 3), qf1, z);
        { const float a = qred(d0[1]), c = qred(d1[1]); P0 = sub == 1 ? a : P0; P1 = sub == 1 ? c : P1; }
        AFTER(base + 3);
        { const float a = qred(d0[2]), c = qred(d1[2]); P0 = sub == 2 ? a : P0; P1 = sub == 2 ? c : P1; }
        { const float a = qred(d0[3]), c = qred(d1[3]); P0 = sub == 3 ? a : P0; P1 = sub == 3 ? c : P1; }
        float t0 = fmaxf(P0, dpp<XOR1>(P0)), t1 = fmaxf(P1, dpp<XOR1>(P1));
        t0 = fmaxf(t0, dpp<XOR2>(t0)); t1 = fmaxf(t1, dpp<XOR2>(t1));
        t0 = x16x32_max(t0) * C2; t1 = x16x32_max(t1) * C2;
        if (r == 0) { m0 = t0; m1 = t1; }
        else if (__any(t0 > m0 || t1 > m1)) {
            const float n0 = fmaxf(m0, t0), n1 = fmaxf(m1, t1);
            const float a0 = __builtin_amdgcn_exp2f(m0 - n0), a1 = __builtin_amdgcn_exp2f(m1 - n1);
            m0 = n0; m1 = n1; l0 *= a0; l1 *= a1;
            const float b0 = __shfl(a0, (lane >> 4) * 20), b1 = __shfl(a1, (lane >> 4) * 20);
            o00 *= b0; o01 *= b0; o10 *= b1; o11 *= b1;
        }
        const float p0 = __builtin_amdgcn_exp2f(fmaf(P0, C2, -m0)), p1 = __builtin_amdgcn_exp2f(fmaf(P1, C2, -m1));
        l0 += p0; l1 += p1;
        const unsigned pp0 = pk(p0, p0), pp1 = pk(p1, p1);
        if (PRIO && r == 0 && wv >= 4) __builtin_amdgcn_s_setprio(1);
#define PV(t) do { \
            const unsigned b0_ = qbc<t>(pp0), b1_ = qbc<t>(pp1); \
            const v2u a0_ = {b0_ & mk0, b0_ & mk1}, a1_ = {b1_ & mk0, b1_ & mk1}; \
            const v4u vf_ = GET(base + TPR + (t)); \
            o00 = mfma16(a0_, v2u{vf_.x, vf_.y}, o00); o01 = mfma16(a0_, v2u{vf_.z, vf_.w}, o01); \
            o10 = mfma16(a1_, v2u{vf_.x, vf_.y}, o10); o11 = mfma16(a1_, v2u{vf_.z, vf_.w}, o11); \
            AFTER(base + TPR + (t)); } while (0)
        PV(0); PV(1); PV(2); PV(3);
        if (r == NROUND / 2 - 1) { STAMP(4); if (wv == 0 && lane == 0) flags[6] = 1u; }
    }
    STAMP(5);
    l0 += dpp<XOR1>(l0); l1 += dpp<XOR1>(l1); l0 += dpp<XOR2>(l0); l1 += dpp<XOR2>(l1);
    l0 = x16x32_sum(l0); l1 = x16x32_sum(l1);
    {
        LAS unsigned char* mb = L + L_MRG + wv * 4096 + (lane >> 4) * 512 + (lane & 15) * 32;
        *(LAS f32x4*)(mb) = o00; *(LAS f32x4*)(mb + 16) = o01;
        *(LAS f32x4*)(mb + 2048) = o10; *(LAS f32x4*)(mb + 2048 + 16) = o11;
        if ((lane & 0x33) == 0) {
            LAS unsigned char* q = L + L_ML + wv * 64 + (lane >> 2) * 8;
            *(LAS f32x2*)(q) = f32x2{m0, dead ? __builtin_nanf("") : l0}; *(LAS f32x2*)(q + 32) = f32x2{m1, l1};
        }
    }
    WG_BARRIER();
    STAMP(6);
    const unsigned tag = flags[3];
    {
        f32x2 ml[NSW];
#pragma unroll
        for (int i = 0; i < NSW; ++i) ml[i] = *(const LAS f32x2*)(L + L_ML + i * 64 + wv * 8);
        float M = ml[0][0];
#pragma unroll
        for (int i = 1; i < NSW; ++i) M = fmaxf(M, ml[i][0]);
        float Ls = 0.f, a0 = 0.f, a1 = 0.f;
#pragma unroll
        for (int i = 0; i < NSW; ++i) {
            const float wt_ = __builtin_amdgcn_exp2f(ml[i][0] - M);
            const f32x2 o = *(const LAS f32x2*)(L + L_MRG + i * 4096 + wv * 512 + lane * 8);
            Ls = fmaf(wt_, ml[i][1], Ls); a0 = fmaf(wt_, o[0], a0); a1 = fmaf(wt_, o[1], a1);
        }
        st_sc1(rs, WS_X1 + (kvh * 64 + wv * 8 + (lane >> 3)) * X1_STRIDE + sp * 128 + (lane & 7) * 16, v4u{fu(a0), tag, fu(a1), tag});
        if (lane == 0) st_sc1(rs, WS_ML + ((kvh * 8 + wv) * 64 + sp) * 16, v4u{fu(M), tag, fu(Ls), tag});
    }
    STAMP(7);
    const f32x2 sc = *(const LAS f32x2*)(L + L_SC);
    f32x4 acc[2] = {{0.f, 0.f, 0.f, 0.f}, {0.f, 0.f, 0.f, 0.f}};
    v4u tl[8];
    {
        const int xo = WS_X1 + w * X1_STRIDE + wv * 1024 + lane * 16;
        const int mo = WS_ML + ((w >> 3) * 64 + wv * 8 + (lane >> 3)) * 16;
        v4u ml = ld_sc1(rs, mo), d = ld_sc1(rs, xo);
        SB();
#pragma unroll
        for (int t = 0; t < 8; ++t) { tl[t] = issue_w(12 + t); }
        SB();
        for (unsigned spins = 0;; ++spins) {
            if (__all(ml.y == tag && ml.w == tag && d.y == tag && d.w == tag)) break;
            if (spins > SPIN_CAP) { dead = true; break; }
            asm volatile("" ::: "memory");
            ml = ld_sc1(rs, mo); d = ld_sc1(rs, xo);
        }
        const float m = uf(ml.x);
        float M = fmaxf(m, dpp<XOR8>(m)); M = x16x32_max(M);
        const float wt_ = __builtin_amdgcn_exp2f(m - M);
        float Ls = wt_ * uf(ml.z), o0 = wt_ * uf(d.x), o1 = wt_ * uf(d.z);
        Ls += dpp<XOR8>(Ls); o0 += dpp<XOR8>(o0); o1 += dpp<XOR8>(o1);
        Ls = x16x32_sum(Ls); o0 = x16x32_sum(o0); o1 = x16x32_sum(o1);
        if (lane < 8) {
            *(LAS float*)(L + L_X1 + 64 + ((2 * lane) * 8 + wv) * 4) = o0;
            *(LAS float*)(L + L_X1 + 64 + ((2 * lane + 1) * 8 + wv) * 4) = o1;
            if (lane == 0) *(LAS f32x2*)(L + L_X1 + wv * 8) = f32x2{M, dead ? __builtin_nanf("") : Ls};
        }
#pragma unroll
        for (int t = 0; t < 4; ++t) {
            const float h = *(const LAS float*)(L + L_HV + (4 * t + (lane >> 4)) * 4) * sc[1];
            rows_mfma(acc, tl[t], pk(h, h), mk0, mk1);
        }
    }
    STAMP(8);
    WG_BARRIER();
    {
        f32x2 ml[NSW];
#pragma unroll
        for (int i = 0; i < NSW; ++i) ml[i] = *(const LAS f32x2*)(L + L_X1 + i * 8);
        float M = ml[0][0];
#pragma unroll
        for (int i = 1; i < NSW; ++i) M = fmaxf(M, ml[i][0]);
        float wt_[NSW], Ls = 0.f;
#pragma unroll
        for (int i = 0; i < NSW; ++i) { wt_[i] = __builtin_amdgcn_exp2f(ml[i][0] - M); Ls = fmaf(wt_[i], ml[i][1], Ls); }
        const float inv = 1.f / Ls;
#pragma unroll
        for (int t = 0; t < 4; ++t) {
            const LAS unsigned char* q = L + L_X1 + 64 + (4 * t + (lane >> 4)) * 32;
            const f32x4 u = *(const LAS f32x4*)(q), v = *(const LAS f32x4*)(q + 16);
            float o = wt_[0] * u[0];
            o = fmaf(wt_[1], u[1], o); o = fmaf(wt_[2], u[2], o); o = fmaf(wt_[3], u[3], o);
            o = fmaf(wt_[4], v[0], o); o = fmaf(wt_[5], v[1], o); o = fmaf(wt_[6], v[2], o); o = fmaf(wt_[7], v[3], o);
            o = rbf(o * inv) * sc[0];
            rows_mfma(acc, tl[4 + t], pk(o, o), mk0, mk1);
        }
        const int g2 = lane >> 4, c = lane & 15;
        const f32x4 a = (g2 & 2) ? acc[1] : acc[0];
        const float v0 = (g2 & 1) ? a[2] : a[0], v1 = (g2 & 1) ? a[3] : a[1];
        const int cons = 8 * wv + (c >> 1);
        st_sc1(rs, WS_X2 + cons * X2_STRIDE + w * 128 + ((c & 1) * 4 + g2) * 16, v4u{fu(v0), tag, fu(v1), tag});
    }
    STAMP(9);
    if ((w & 3) == 0) {
        const int xo = WS_X2 + (w >> 2) * X2_STRIDE + wv * 4096 + lane * 16;
        const unsigned xv = *(const GAS unsigned*)(p.x + 16 * (w >> 2) + 2 * (lane & 7));
        v4u d[4];
#pragma unroll
        for (int i = 0; i < 4; ++i) d[i] = ld_sc1(rs, xo + i * 1024);
        for (unsigned spins = 0;; ++spins) {
            unsigned pend = 0u;
#pragma unroll
            for (int i = 0; i < 4; ++i) if (!__all(d[i].y == tag && d[i].w == tag)) pend |= 1u << i;
            if (pend == 0u) break;
            if (spins > SPIN_CAP) { dead = true; break; }
            asm volatile("" ::: "memory");
#pragma unroll
            for (int i = 0; i < 4; ++i) if ((pend >> i) & 1u) d[i] = ld_sc1(rs, xo + i * 1024);
        }
        float s0 = (uf(d[0].x) + uf(d[1].x)) + (uf(d[2].x) + uf(d[3].x)), s1 = (uf(d[0].z) + uf(d[1].z)) + (uf(d[2].z) + uf(d[3].z));
        s0 += dpp<XOR8>(s0); s1 += dpp<XOR8>(s1);
        s0 = x16x32_sum(s0); s1 = x16x32_sum(s1);
        if (dead) s0 = __builtin_nanf("");
        if (lane < 8) *(LAS f32x2*)(L + L_X2 + wv * 64 + lane * 8) = f32x2{s0, s1};
        STAMP(10);
        WG_BARRIER();
        if (wv == 0 && lane < 8) {
            f32x2 s = {0.f, 0.f};
#pragma unroll
            for (int i = 0; i < NSW; ++i) s += *(const LAS f32x2*)(L + L_X2 + i * 64 + lane * 8);
            const int col = 16 * (w >> 2) + 2 * lane;
            *(GAS unsigned*)(p.out + col) = pk(fmaf(1.f - sc[0], bflo(xv), s[0]), fmaf(1.f - sc[0], bfhi(xv), s[1]));
            if (w == 0 && lane == 0) __builtin_amdgcn_raw_buffer_store_b32(tag, rs, WS_EPOCH, 0, AUX_SC1);
        }
        STAMP(11);
    }
    STAMP_FLUSH();
}

extern "C" void kernel_launch(void* const* d_in, const int* in_sizes, int n_in, void* d_out, int out_size, void* d_ws, size_t ws_size,
                              hipStream_t stream) {
    Params p;
    p.x = (const unsigned short*)d_in[0];
    p.kc = (const unsigned short*)d_in[1];
    p.vc = (const unsigned short*)d_in[2];
    p.wq = (const unsigned short*)d_in[3];
    p.wo = (const unsigned short*)d_in[4];
    p.wr = (const unsigned short*)d_in[6];
    p.wg = (const unsigned short*)d_in[7];
    p.wu = (const unsigned short*)d_in[8];
    p.wd = (const unsigned short*)d_in[9];
    p.gw = (const unsigned short*)d_in[10];
    p.out = (unsigned short*)d_out;
    p.ws = (unsigned char*)d_ws;
    hipLaunchKernelGGL(fused_decode_kernel, dim3(NWG), dim3(NTHR), LDS_BYTES, stream, p);
}
```

```cpp
#include <hip/hip_runtime.h>
#include <hip/hip_bf16.h>
#include <stdint.h>

#define GAS __attribute__((address_space(1)))
#define LAS __attribute__((address_space(3)))

typedef unsigned v4u __attribute__((ext_vector_type(4)));
typedef unsigned v2u __attribute__((ext_vector_type(2)));
typedef float f32x4 __attribute__((ext_vector_type(4)));
typedef float f32x2 __attribute__((ext_vector_type(2)));
typedef short bf16x8 __attribute__((ext_vector_type(8)));
typedef short bf16x4 __attribute__((ext_vector_type(4)));
typedef __bf16 bf2 __attribute__((ext_vector_type(2)));

namespace {

constexpr int NWG = 256, NSW = 8, NTHR = (NSW + 1) * 64;
constexpr int DM = 1024, SK = 65536, DH = 128, FF = 3584, NQ = 4096;
#define EARLY 2
#define TAILD 4
#define TAILDM1 3
#define PRIO 0
#define DEPTH 6
#define DTW 28
#define PROG1 4
#define PROG2 13
constexpr int TPR = 4, NROUND = 8;
constexpr int E_END = 64;
constexpr int DT = DTW;
constexpr int jmax(int e) {
    const int a = DT - 1 + (e + 2) / 2, b = e + DEPTH + 1;
    const int m = a > b ? a : b;
    return m < E_END - 1 ? m : E_END - 1;
}

constexpr int WS_EPOCH = 0;
constexpr int WS_Q = 4096;
constexpr int WS_Q1 = WS_Q + 2048 * 8;
constexpr int Q1_STRIDE = 32 * 128;
constexpr int WS_ML = WS_Q1 + NWG * Q1_STRIDE;
constexpr int WS_X1 = WS_ML + 32 * 64 * 16;
constexpr int X1_STRIDE = 64 * 128;
constexpr int WS_X2 = WS_X1 + NWG * X1_STRIDE;
constexpr int X2_STRIDE = NWG * 128;
constexpr int WS_H = WS_X2 + 64 * X2_STRIDE;
constexpr int H_STRIDE = 32 * 256;
constexpr int WS_END = WS_H + NWG * H_STRIDE;

constexpr int L_FLAGS = 0;
constexpr int L_HV = 128;
constexpr int L_SC = 192;
constexpr int L_QF = 256;
constexpr int L_ML = L_QF + 2048;
constexpr int L_X1 = L_ML + 512;
constexpr int L_X2 = L_X1 + 64 + 512;
constexpr int L_GU = L_X2 + 512;
constexpr int L_QP = L_GU + 8 * 2 * 448 * 4;
constexpr int L_MRG = L_QP + 8 * 512 * 4;
constexpr int L_END = L_MRG + 8 * 8 * 128 * 4;
constexpr int LDS_BYTES = 88 * 1024;
static_assert(L_END <= LDS_BYTES, "lds");
static_assert(LDS_BYTES <= 160 * 1024 && LDS_BYTES > 80 * 1024, "lds");
static_assert(L_MRG % 16 == 0 && L_GU % 16 == 0 && L_QP % 16 == 0, "align");

constexpr unsigned SPIN_CAP = 1u << 18;
constexpr int AUX_SC1 = 16;

struct Params {
    const unsigned short* x;
    const unsigned short* kc;
    const unsigned short* vc;
    const unsigned short* wq;
    const unsigned short* wo;
    const unsigned short* wr;
    const unsigned short* wg;
    const unsigned short* wu;
    const unsigned short* wd;
    const unsigned short* gw;
    unsigned short* out;
    unsigned char* ws;
};

__device__ __forceinline__ v4u ldnt(const unsigned char* p) { return __builtin_nontemporal_load((const GAS v4u*)p); }
__device__ __forceinline__ float bflo(unsigned u) { return __builtin_bit_cast(float, u << 16); }
__device__ __forceinline__ float bfhi(unsigned u) { return __builtin_bit_cast(float, u & 0xffff0000u); }
__device__ __forceinline__ unsigned pk(float lo, float hi) {
    bf2 r = {(__bf16)lo, (__bf16)hi};
    return __builtin_bit_cast(unsigned, r);
}
__device__ __forceinline__ float rbf(float v) { return bflo(pk(v, v)); }
__device__ __forceinline__ unsigned fu(float f) { return __builtin_bit_cast(unsigned, f); }
__device__ __forceinline__ float uf(unsigned u) { return __builtin_bit_cast(float, u); }
template <int CTRL> __device__ __forceinline__ float dpp(float x) {
    return uf((unsigned)__builtin_amdgcn_mov_dpp((int)fu(x), CTRL, 0xf, 0xf, true));
}
constexpr int XOR1 = 0xB1, XOR2 = 0x4E, XOR7 = 0x141, XOR8 = 0x128, ROR4 = 0x124;
__device__ __forceinline__ float swap32_add(float a, float b) {
    auto s = __builtin_amdgcn_permlane32_swap(fu(a), fu(b), false, false);
    return uf(s[0]) + uf(s[1]);
}
__device__ __forceinline__ float swap16_add(float a, float b) {
    auto s = __builtin_amdgcn_permlane16_swap(fu(a), fu(b), false, false);
    return uf(s[0]) + uf(s[1]);
}
__device__ __forceinline__ float x16x32_max(float x) {
    auto s = __builtin_amdgcn_permlane16_swap(fu(x), fu(x), false, false);
    x = fmaxf(uf(s[0]), uf(s[1]));
    auto t = __builtin_amdgcn_permlane32_swap(fu(x), fu(x), false, false);
    return fmaxf(uf(t[0]), uf(t[1]));
}
__device__ __forceinline__ float x16x32_sum(float x) { x = swap16_add(x, x); return swap32_add(x, x); }
__device__ __forceinline__ float row_sum(float x) {
    x += dpp<XOR1>(x); x += dpp<XOR2>(x); x += dpp<XOR7>(x); x += dpp<XOR8>(x); return x;
}
__device__ __forceinline__ float row_max(float x) {
    x = fmaxf(x, dpp<XOR1>(x)); x = fmaxf(x, dpp<XOR2>(x)); x = fmaxf(x, dpp<XOR7>(x)); x = fmaxf(x, dpp<XOR8>(x)); return x;
}
__device__ __forceinline__ float wave_sum(float x) { return x16x32_sum(row_sum(x)); }
__device__ __forceinline__ float wave_max(float x) { return x16x32_max(row_max(x)); }

__device__ __forceinline__ f32x4 mfma32(v4u a, v4u b, f32x4 c) {
    return __builtin_amdgcn_mfma_f32_16x16x32_bf16(__builtin_bit_cast(bf16x8, a), __builtin_bit_cast(bf16x8, b), c, 0, 0, 0);
}
__device__ __forceinline__ f32x4 mfma16(v2u a, v2u b, f32x4 c) {
    return __builtin_amdgcn_mfma_f32_16x16x16bf16_1k(__builtin_bit_cast(bf16x4, a), __builtin_bit_cast(bf16x4, b), c, 0, 0, 0);
}
#define WG_BARRIER() do { asm volatile("s_waitcnt lgkmcnt(0)" ::: "memory"); __builtin_amdgcn_s_barrier(); asm volatile("" ::: "memory"); } while (0)
#define SB() __builtin_amdgcn_sched_barrier(0)
#define SBMASK 0x10F
#define SBM() __builtin_amdgcn_sched_barrier(SBMASK)
#define STR2(x) #x
#define STR(x) STR2(x)
#define THROTTLE() asm volatile("s_waitcnt vmcnt(" STR(DEPTHM1) ")" ::: "memory")
#define DEPTHM1 5

__device__ __forceinline__ void fma8(float (&acc)[8], v4u wv, float xv) {
    acc[0] = fmaf(xv, bflo(wv.x), acc[0]); acc[1] = fmaf(xv, bfhi(wv.x), acc[1]);
    acc[2] = fmaf(xv, bflo(wv.y), acc[2]); acc[3] = fmaf(xv, bfhi(wv.y), acc[3]);
    acc[4] = fmaf(xv, bflo(wv.z), acc[4]); acc[5] = fmaf(xv, bfhi(wv.z), acc[5]);
    acc[6] = fmaf(xv, bflo(wv.w), acc[6]); acc[7] = fmaf(xv, bfhi(wv.w), acc[7]);
}
__device__ __forceinline__ f32x2 reduce_cols(const float (&a)[8]) {
    const float b0 = swap32_add(a[0], a[4]), b1 = swap32_add(a[1], a[5]), b2 = swap32_add(a[2], a[6]), b3 = swap32_add(a[3], a[7]);
    float c0 = swap16_add(b0, b2), c1 = swap16_add(b1, b3);
    c0 += dpp<XOR2>(c0); c1 += dpp<XOR2>(c1);
    c0 += dpp<ROR4>(c0); c1 += dpp<ROR4>(c1);
    c0 += dpp<XOR8>(c0); c1 += dpp<XOR8>(c1);
    return f32x2{c0, c1};
}

__device__ __forceinline__ v4u ld_sc1(__amdgpu_buffer_rsrc_t rs, int off) { return __builtin_amdgcn_raw_buffer_load_b128(rs, off, 0, AUX_SC1); }
__device__ __forceinline__ void st_sc1(__amdgpu_buffer_rsrc_t rs, int off, v4u v) { __builtin_amdgcn_raw_buffer_store_b128(v, rs, off, 0, AUX_SC1); }

__device__ __forceinline__ void rows_mfma(f32x4 (&acc)[2], v4u wv, unsigned pp, unsigned m0, unsigned m1) {
    const v2u a = {pp & m0, pp & m1};
    acc[0] = mfma16(a, v2u{wv.x, wv.y}, acc[0]);
    acc[1] = mfma16(a, v2u{wv.z, wv.w}, acc[1]);
}
__device__ __forceinline__ unsigned lds_arrive(LAS unsigned char* L, int word, int lane) {
    asm volatile("s_waitcnt lgkmcnt(0)" ::: "memory");
    unsigned old = 0;
    if (lane == 0) old = __hip_atomic_fetch_add((LAS unsigned*)(L + L_FLAGS + 4 * word), 1u, __ATOMIC_RELAXED, __HIP_MEMORY_SCOPE_WORKGROUP);
    old = __builtin_amdgcn_readfirstlane(old);
    asm volatile("" ::: "memory");
    return old;
}
__device__ __forceinline__ void lds_wait_slow(volatile LAS unsigned* flags, int word, unsigned want) {
    for (unsigned spins = 0; flags[word] < want && spins < SPIN_CAP; ++spins) __builtin_amdgcn_s_sleep(8);
    asm volatile("" ::: "memory");
}
__device__ __forceinline__ void lds_wait(volatile LAS unsigned* flags, int word, unsigned want) {
    for (unsigned spins = 0; flags[word] < want && spins < (SPIN_CAP << 4); ++spins) __builtin_amdgcn_s_sleep(1);
    asm volatile("" ::: "memory");
}
__device__ __forceinline__ float qred(f32x4 d) {
    float t = dpp<0x00>(d[0]);
    t = dpp<0x55>(d[1]) + t;
    asm volatile("" : "+v"(t));
    t = dpp<0xAA>(d[2]) + t;
    t = dpp<0xFF>(d[3]) + t;
    return t;
}
template <int T> __device__ __forceinline__ unsigned qbc(unsigned x) {
    return (unsigned)__builtin_amdgcn_mov_dpp((int)x, T * 0x55, 0xf, 0xf, true);
}

#define STAMP(i) do {} while (0)
#define STAMP_DECL() do {} while (0)
#define STAMP_FLUSH() do {} while (0)

}

__global__ void __launch_bounds__(NTHR, 1) fused_decode_kernel(Params p) {
    extern __shared__ __attribute__((aligned(16))) unsigned char smem[];
    LAS unsigned char* L = (LAS unsigned char*)smem;
    volatile LAS unsigned* flags = (volatile LAS unsigned*)(L + L_FLAGS);
    const int tid = threadIdx.x, lane = tid & 63;
    const int wv = __builtin_amdgcn_readfirstlane(tid >> 6);
    const int b = blockIdx.x;
    const int w = (b & 7) * 32 + (b >> 3);
    const int kvh = w >> 6, sp = w & 63;
    const int cb = w & 7, rb = w >> 3;
    const __amdgpu_buffer_rsrc_t rs = __builtin_amdgcn_make_buffer_rsrc((void*)p.ws, 0, WS_END, 0x00020000);
    STAMP_DECL();
    STAMP(0);

    if (wv == NSW) {
        const unsigned ep = __builtin_amdgcn_raw_buffer_load_b32(rs, WS_EPOCH, 0, AUX_SC1);
        if (lane < 8) flags[lane] = 0u;
        WG_BARRIER();
        const unsigned tag = __builtin_amdgcn_readfirstlane(ep) + 1u;
        if (lane == 0) flags[3] = tag;
        bool dead = false;
        STAMP(1);
        lds_wait(flags, 1, NSW);
#pragma unroll
        for (int it = 0; it < 4; ++it) {
            const int c = it * 128 + lane * 2;
            f32x2 s = {0.f, 0.f};
#pragma unroll
            for (int i = 0; i < NSW; ++i) s += *(const LAS f32x2*)(L + L_QP + (i * 512 + c) * 4);
            st_sc1(rs, WS_Q1 + (cb * 32 + (c >> 4)) * Q1_STRIDE + rb * 128 + (c & 15) * 8, v4u{fu(s[0]), tag, fu(s[1]), tag});
        }
        STAMP(2);
        lds_wait_slow(flags, 4, 1u);
        {
            const int xo = WS_Q1 + w * Q1_STRIDE;
            v4u d[4];
            for (unsigned spins = 0;; ++spins) {
                asm volatile("" ::: "memory");
#pragma unroll
                for (int i = 0; i < 4; ++i) d[i] = ld_sc1(rs, xo + i * 1024 + lane * 16);
                bool ok = true;
#pragma unroll
                for (int i = 0; i < 4; ++i) ok = ok && d[i].y == tag && d[i].w == tag;
                if (__all(ok)) break;
                if (spins > SPIN_CAP) { dead = true; break; }
                __builtin_amdgcn_s_sleep(8);
            }
            float s0 = (uf(d[0].x) + uf(d[1].x)) + (uf(d[2].x) + uf(d[3].x)), s1 = (uf(d[0].z) + uf(d[1].z)) + (uf(d[2].z) + uf(d[3].z));
            s0 += dpp<XOR8>(s0); s1 += dpp<XOR8>(s1);
            s0 = x16x32_sum(s0); s1 = x16x32_sum(s1);
            const unsigned pr = pk(s0, s1);
            const unsigned nx = __builtin_amdgcn_mov_dpp((int)pr, XOR1, 0xf, 0xf, true);
            if (lane < 8 && !(lane & 1) && !dead) st_sc1(rs, WS_Q + (w * 8 + lane) * 8, v4u{pr, tag, nx, tag});
        }
        STAMP(3);
        lds_wait(flags, 2, NSW);
#pragma unroll
        for (int it = 0; it < 7; ++it) {
            const int idx = it * 64 + lane, j = idx / 14, k = idx - 14 * j;
            const int mat = k >= 7 ? 1 : 0, u = 14 * j + 2 * (k - 7 * mat);
            f32x2 s = {0.f, 0.f};
#pragma unroll
            for (int i = 0; i < NSW; ++i) s += *(const LAS f32x2*)(L + L_GU + ((i * 2 + mat) * 448 + u) * 4);
            st_sc1(rs, WS_H + (cb * 32 + j) * H_STRIDE + rb * 256 + k * 16, v4u{fu(s[0]), tag, fu(s[1]), tag});
        }
        lds_wait_slow(flags, 5, 1u);
        {
            const int qoff = WS_Q + (kvh * 512 + ((lane >> 2) & 3) * 64 + (4 * (lane >> 4) + (lane & 3)) * 4) * 8;
            v4u g0, g1, g2, g3;
            for (unsigned spins = 0;; ++spins) {
                asm volatile("" ::: "memory");
                g0 = ld_sc1(rs, qoff); g1 = ld_sc1(rs, qoff + 16); g2 = ld_sc1(rs, qoff + 2048); g3 = ld_sc1(rs, qoff + 2048 + 16);
                const bool ok = g0.y == tag && g0.w == tag && g1.y == tag && g1.w == tag && g2.y == tag && g2.w == tag && g3.y == tag && g3.w == tag;
                if (__all(ok) || dead) break;
                if (spins > SPIN_CAP) { dead = true; break; }
                __builtin_amdgcn_s_sleep(4);
            }
            *(LAS v4u*)(L + L_QF + lane * 16) = v4u{g0.x, g0.z, g1.x, g1.z};
            *(LAS v4u*)(L + L_QF + 1024 + lane * 16) = v4u{g2.x, g2.z, g3.x, g3.z};
            asm volatile("s_waitcnt lgkmcnt(0)" ::: "memory");
            if (lane == 0) flags[0] = dead ? 2u : 1u;
        }
        STAMP(4);
        {
            const GAS v4u* xp = (const GAS v4u*)p.x; const GAS v4u* gp = (const GAS v4u*)p.gw; const GAS v4u* rp = (const GAS v4u*)p.wr;
            float sg = 0.f, sr = 0.f;
#pragma unroll
            for (int i = 0; i < 2; ++i) {
                const v4u xv = xp[lane + 64 * i], gv = gp[lane + 64 * i], rv = rp[lane + 64 * i];
#pragma unroll
                for (int k = 0; k < 4; ++k) {
                    sg += rbf(bflo(xv[k]) * bflo(gv[k])); sg += rbf(bfhi(xv[k]) * bfhi(gv[k]));
                    sr = fmaf(bflo(xv[k]), bflo(rv[k]), sr); sr = fmaf(bfhi(xv[k]), bfhi(rv[k]), sr);
                }
            }
            sg = rbf(wave_sum(sg)); sr = rbf(wave_sum(sr));
            const float g_gate = rbf(1.f / (1.f + __expf(-sg)));
            const float rsg = rbf(1.f / (1.f + __expf(-sr)));
            if (lane == 0) *(LAS f32x2*)(L + L_SC) = f32x2{g_gate, (1.f - g_gate) * rsg};
        }
        lds_wait_slow(flags, 6, 1u);
        {
            const int xo = WS_H + w * H_STRIDE;
            const bool used = (lane & 15) < 14;
            v4u d[8];
            for (unsigned spins = 0;; ++spins) {
                asm volatile("" ::: "memory");
#pragma unroll
                for (int i = 0; i < 8; ++i) d[i] = ld_sc1(rs, xo + i * 1024 + lane * 16);
                bool ok = true;
#pragma unroll
                for (int i = 0; i < 8; ++i) ok = ok && d[i].y == tag && d[i].w == tag;
                if (__all(ok || !used) || dead) break;
                if (spins > SPIN_CAP) { dead = true; break; }
                __builtin_amdgcn_s_sleep(32);
            }
            float s0 = 0.f, s1 = 0.f;
#pragma unroll
            for (int i = 0; i < 8; ++i) { s0 += uf(d[i].x); s1 += uf(d[i].z); }
            s0 = rbf(x16x32_sum(s0)); s1 = rbf(x16x32_sum(s1));
            const float u0 = __shfl(s0, (lane & 15) + 7), u1 = __shfl(s1, (lane & 15) + 7);
            const float a0 = rbf(s0 / (1.f + __expf(-s0))), a1 = rbf(s1 / (1.f + __expf(-s1)));
            float h0 = rbf(a0 * u0), h1 = rbf(a1 * u1);
            if (lane >= 7) { h0 = 0.f; h1 = 0.f; }
            if (dead) h0 = __builtin_nanf("");
            if (lane < 8) *(LAS f32x2*)(L + L_HV + lane * 8) = f32x2{h0, h1};
        }
        STAMP(5);
        WG_BARRIER();
        STAMP_FLUSH();
        return;
    }

    const int grow = 32 * rb + 4 * wv;
    const unsigned char* wq_p = (const unsigned char*)p.wq + (size_t)grow * (NQ * 2) + cb * 1024 + lane * 16;
    const size_t kv_base = ((size_t)kvh * SK + (size_t)sp * 1024 + wv * 4) * 256;
    const unsigned char* k_p = (const unsigned char*)p.kc + kv_base + ((lane >> 2) & 3) * 256 + (4 * (lane >> 4) + (lane & 3)) * 16;
    const unsigned char* v_p = (const unsigned char*)p.vc + kv_base + lane * 16;
    const int glane = lane < 56 ? lane : 55;
    const size_t goff = (size_t)grow * (FF * 2) + cb * 896 + glane * 16;
    const unsigned char* wg_p = (const unsigned char*)p.wg + goff;
    const unsigned char* wu_p = (const unsigned char*)p.wu + goff;
    const unsigned char* wd_p = (const unsigned char*)p.wd + (size_t)(w * 14) * (DM * 2) + wv * 256 + (lane & 15) * 16;
    const unsigned char* wo_p = (const unsigned char*)p.wo + (size_t)(w * 16 + (lane >> 4)) * (DM * 2) + wv * 256 + (lane & 15) * 16;

    auto issue_w = [&](int i) -> v4u {
        if (i < 4) return ldnt(wq_p + (size_t)i * (NQ * 2));
        if (i < 8) return ldnt(wg_p + (size_t)(i - 4) * (FF * 2));
        if (i < 12) return ldnt(wu_p + (size_t)(i - 8) * (FF * 2));
        if (i < 16) { const int r = 4 * (i - 12) + (lane >> 4); return ldnt(wd_p + (size_t)(r < 14 ? r : 13) * (DM * 2)); }
        return ldnt(wo_p + (size_t)(i - 16) * 4 * (DM * 2));
    };
    auto issue = [&](int e) -> v4u {
        const int r = e / (2 * TPR), k = e % (2 * TPR);
        return k < TPR ? ldnt(k_p + (size_t)(r * TPR + k) * 8192) : ldnt(v_p + (size_t)(r * TPR + k - TPR) * 8192);
    };
    v4u ring[DT];
#define ISSUE(j) do { THROTTLE(); ring[(j) % DT] = issue(j); SB(); } while (0)
#define AFTER(e) do { if (jmax(e) > ((e) == 0 ? DT - 1 : jmax((e) - 1))) ISSUE(jmax(e)); } while (0)
#define GET(e) ring[(e) % DT]

    const v2u xg = *(const GAS v2u*)(p.x + grow);
    v4u wt[12];
#pragma unroll
    for (int i = 0; i < 4; ++i) wt[i] = issue_w(i);
    SB();
    WG_BARRIER();
#pragma unroll
    for (int i = 4; i < 4 + EARLY; ++i) wt[i] = issue_w(i);
    SB();
    bool dead = false;
    {
        const float xv[4] = {bflo(xg.x), bfhi(xg.x), bflo(xg.y), bfhi(xg.y)};
        float acc[8] = {0.f, 0.f, 0.f, 0.f, 0.f, 0.f, 0.f, 0.f};
#pragma unroll
        for (int t = 0; t < 4; ++t) fma8(acc, wt[t], xv[t]);
        {
            LAS unsigned char* q = L + L_QP + (wv * 512 + lane * 8) * 4;
            *(LAS f32x4*)(q) = f32x4{acc[0], acc[1], acc[2], acc[3]}; *(LAS f32x4*)(q + 16) = f32x4{acc[4], acc[5], acc[6], acc[7]};
        }
        lds_arrive(L, 1, lane);
        STAMP(1);
        float ag[8] = {0.f, 0.f, 0.f, 0.f, 0.f, 0.f, 0.f, 0.f}, au[8] = {0.f, 0.f, 0.f, 0.f, 0.f, 0.f, 0.f, 0.f};
#pragma unroll
        for (int t = 4 + EARLY; t < 12; ++t) { THROTTLE(); wt[t] = issue_w(t); SB(); }
#pragma unroll
        for (int j = 0; j < 4; ++j) ISSUE(j);
#pragma unroll
        for (int t = 0; t < 4; ++t) { fma8(ag, wt[4 + t], xv[t]); }
#pragma unroll
        for (int t = 0; t < 4; ++t) { fma8(au, wt[8 + t], xv[t]); }
        if (lane < 56) {
            LAS unsigned char* q = L + L_GU + (wv * 2 * 448 + lane * 8) * 4;
            *(LAS f32x4*)(q) = f32x4{ag[0], ag[1], ag[2], ag[3]}; *(LAS f32x4*)(q + 16) = f32x4{ag[4], ag[5], ag[6], ag[7]};
            *(LAS f32x4*)(q + 1792) = f32x4{au[0], au[1], au[2], au[3]}; *(LAS f32x4*)(q + 1792 + 16) = f32x4{au[4], au[5], au[6], au[7]};
        }
        lds_arrive(L, 2, lane);
    }
#pragma unroll
    for (int j = 4; j < DT; ++j) {
        ISSUE(j);
        if (j == PROG1 && wv == 0 && lane == 0) flags[4] = 1u;
        if (j == PROG2 && wv == 0 && lane == 0) flags[5] = 1u;
    }
    STAMP(2);
    lds_wait(flags, 0, 1u);
    if (flags[0] != 1u) dead = true;
    const v4u qf0 = *(const LAS v4u*)(L + L_QF + lane * 16), qf1 = *(const LAS v4u*)(L + L_QF + 1024 + lane * 16);
    STAMP(3);

    constexpr float C2 = 0.088388347648318440f * 1.4426950408889634f;
    const unsigned mk0 = (lane & 3) == 0 ? 0x0000ffffu : (lane & 3) == 1 ? 0xffff0000u : 0u;
    const unsigned mk1 = (lane & 3) == 2 ? 0x0000ffffu : (lane & 3) == 3 ? 0xffff0000u : 0u;
    const int sub = lane & 3;
    f32x4 o00 = {0.f, 0.f, 0.f, 0.f}, o01 = o00, o10 = o00, o11 = o00;
    float m0 = 0.f, m1 = 0.f, l0 = 0.f, l1 = 0.f;
#pragma unroll
    for (int r = 0; r < NROUND; ++r) {
        const int base = r * 2 * TPR;
        const f32x4 z = {0.f, 0.f, 0.f, 0.f};
        f32x4 d0[TPR], d1[TPR];
        float P0, P1;
        d0[0] = mfma32(GET(base + 0), qf0, z); d1[0] = mfma32(GET(base + 0), qf1, z); AFTER(base + 0);
        d0[1] = mfma32(GET(base + 1), qf0, z); d1[1] = mfma32(GET(base + 1), qf1, z); AFTER(base + 1);
        d0[2] = mfma32(GET(base + 2), qf0, z); d1[2] = mfma32(GET(base + 2), qf1, z);
        P0 = qred(d0[0]); P1 = qred(d1[0]);
        AFTER(base + 2);
        d0[3] = mfma32(GET(base + 3), qf0, z); d1[3] = mfma32(GET(base + 3), qf1, z);
        { const float a = qred(d0[1]), c = qred(d1[1]); P0 = sub == 1 ? a : P0; P1 = sub == 1 ? c : P1; }
        AFTER(base + 3);
        { const float a = qred(d0[2]), c = qred(d1[2]); P0 = sub == 2 ? a : P0; P1 = sub == 2 ? c : P1; }
        { const float a = qred(d0[3]), c = qred(d1[3]); P0 = sub == 3 ? a : P0; P1 = sub == 3 ? c : P1; }
        float t0 = fmaxf(P0, dpp<XOR1>(P0)), t1 = fmaxf(P1, dpp<XOR1>(P1));
        t0 = fmaxf(t0, dpp<XOR2>(t0)); t1 = fmaxf(t1, dpp<XOR2>(t1));
        t0 = x16x32_max(t0) * C2; t1 = x16x32_max(t1) * C2;
        if (r == 0) { m0 = t0; m1 = t1; }
        else if (__any(t0 > m0 || t1 > m1)) {
            const float n0 = fmaxf(m0, t0), n1 = fmaxf(m1, t1);
            const float a0 = __builtin_amdgcn_exp2f(m0 - n0), a1 = __builtin_amdgcn_exp2f(m1 - n1);
            m0 = n0; m1 = n1; l0 *= a0; l1 *= a1;
            const float b0 = __shfl(a0, (lane >> 4) * 20), b1 = __shfl(a1, (lane >> 4) * 20);
            o00 *= b0; o01 *= b0; o10 *= b1; o11 *= b1;
        }
        const float p0 = __builtin_amdgcn_exp2f(fmaf(P0, C2, -m0)), p1 = __builtin_amdgcn_exp2f(fmaf(P1, C2, -m1));
        l0 += p0; l1 += p1;
        const unsigned pp0 = pk(p0, p0), pp1 = pk(p1, p1);
        if (PRIO && r == 0 && wv >= 4) __builtin_amdgcn_s_setprio(1);
#define PV(t) do { \
            const unsigned b0_ = qbc<t>(pp0), b1_ = qbc<t>(pp1); \
            const v2u a0_ = {b0_ & mk0, b0_ & mk1}, a1_ = {b1_ & mk0, b1_ & mk1}; \
            const v4u vf_ = GET(base + TPR + (t)); \
            o00 = mfma16(a0_, v2u{vf_.x, vf_.y}, o00); o01 = mfma16(a0_, v2u{vf_.z, vf_.w}, o01); \
            o10 = mfma16(a1_, v2u{vf_.x, vf_.y}, o10); o11 = mfma16(a1_, v2u{vf_.z, vf_.w}, o11); \
            AFTER(base + TPR + (t)); } while (0)
        PV(0); PV(1); PV(2); PV(3);
        if (r == NROUND / 2 - 1) { STAMP(4); if (wv == 0 && lane == 0) flags[6] = 1u; }
    }
    STAMP(5);
    l0 += dpp<XOR1>(l0); l1 += dpp<XOR1>(l1); l0 += dpp<XOR2>(l0); l1 += dpp<XOR2>(l1);
    l0 = x16x32_sum(l0); l1 = x16x32_sum(l1);
    {
        LAS unsigned char* mb = L + L_MRG + wv * 4096 + (lane >> 4) * 512 + (lane & 15) * 32;
        *(LAS f32x4*)(mb) = o00; *(LAS f32x4*)(mb + 16) = o01;
        *(LAS f32x4*)(mb + 2048) = o10; *(LAS f32x4*)(mb + 2048 + 16) = o11;
        if ((lane & 0x33) == 0) {
            LAS unsigned char* q = L + L_ML + wv * 64 + (lane >> 2) * 8;
            *(LAS f32x2*)(q) = f32x2{m0, dead ? __builtin_nanf("") : l0}; *(LAS f32x2*)(q + 32) = f32x2{m1, l1};
        }
    }
    WG_BARRIER();
    STAMP(6);
    const unsigned tag = flags[3];
    {
        f32x2 ml[NSW];
#pragma unroll
        for (int i = 0; i < NSW; ++i) ml[i] = *(const LAS f32x2*)(L + L_ML + i * 64 + wv * 8);
        float M = ml[0][0];
#pragma unroll
        for (int i = 1; i < NSW; ++i) M = fmaxf(M, ml[i][0]);
        float Ls = 0.f, a0 = 0.f, a1 = 0.f;
#pragma unroll
        for (int i = 0; i < NSW; ++i) {
            const float wt_ = __builtin_amdgcn_exp2f(ml[i][0] - M);
            const f32x2 o = *(const LAS f32x2*)(L + L_MRG + i * 4096 + wv * 512 + lane * 8);
            Ls = fmaf(wt_, ml[i][1], Ls); a0 = fmaf(wt_, o[0], a0); a1 = fmaf(wt_, o[1], a1);
        }
        st_sc1(rs, WS_X1 + (kvh * 64 + wv * 8 + (lane >> 3)) * X1_STRIDE + sp * 128 + (lane & 7) * 16, v4u{fu(a0), tag, fu(a1), tag});
        if (lane == 0) st_sc1(rs, WS_ML + ((kvh * 8 + wv) * 64 + sp) * 16, v4u{fu(M), tag, fu(Ls), tag});
    }
    STAMP(7);
    const f32x2 sc = *(const LAS f32x2*)(L + L_SC);
    f32x4 acc[2] = {{0.f, 0.f, 0.f, 0.f}, {0.f, 0.f, 0.f, 0.f}};
    v4u tl[8];
    {
        const int xo = WS_X1 + w * X1_STRIDE + wv * 1024 + lane * 16;
        const int mo = WS_ML + ((w >> 3) * 64 + wv * 8 + (lane >> 3)) * 16;
        v4u ml = ld_sc1(rs, mo), d = ld_sc1(rs, xo);
        SB();
#pragma unroll
        for (int t = 0; t < 8; ++t) { tl[t] = issue_w(12 + t); }
        SB();
        for (unsigned spins = 0;; ++spins) {
            if (__all(ml.y == tag && ml.w == tag && d.y == tag && d.w == tag)) break;
            if (spins > SPIN_CAP) { dead = true; break; }
            asm volatile("" ::: "memory");
            ml = ld_sc1(rs, mo); d = ld_sc1(rs, xo);
        }
        const float m = uf(ml.x);
        float M = fmaxf(m, dpp<XOR8>(m)); M = x16x32_max(M);
        const float wt_ = __builtin_amdgcn_exp2f(m - M);
        float Ls = wt_ * uf(ml.z), o0 = wt_ * uf(d.x), o1 = wt_ * uf(d.z);
        Ls += dpp<XOR8>(Ls); o0 += dpp<XOR8>(o0); o1 += dpp<XOR8>(o1);
        Ls = x16x32_sum(Ls); o0 = x16x32_sum(o0); o1 = x16x32_sum(o1);
        if (lane < 8) {
            *(LAS float*)(L + L_X1 + 64 + ((2 * lane) * 8 + wv) * 4) = o0;
            *(LAS float*)(L + L_X1 + 64 + ((2 * lane + 1) * 8 + wv) * 4) = o1;
            if (lane == 0) *(LAS f32x2*)(L + L_X1 + wv * 8) = f32x2{M, dead ? __builtin_nanf("") : Ls};
        }
#pragma unroll
        for (int t = 0; t < 4; ++t) {
            const float h = *(const LAS float*)(L + L_HV + (4 * t + (lane >> 4)) * 4) * sc[1];
            rows_mfma(acc, tl[t], pk(h, h), mk0, mk1);
        }
    }
    STAMP(8);
    WG_BARRIER();
    {
        f32x2 ml[NSW];
#pragma unroll
        for (int i = 0; i < NSW; ++i) ml[i] = *(const LAS f32x2*)(L + L_X1 + i * 8);
        float M = ml[0][0];
#pragma unroll
        for (int i = 1; i < NSW; ++i) M = fmaxf(M, ml[i][0]);
        float wt_[NSW], Ls = 0.f;
#pragma unroll
        for (int i = 0; i < NSW; ++i) { wt_[i] = __builtin_amdgcn_exp2f(ml[i][0] - M); Ls = fmaf(wt_[i], ml[i][1], Ls); }
        const float inv = 1.f / Ls;
#pragma unroll
        for (int t = 0; t < 4; ++t) {
            const LAS unsigned char* q = L + L_X1 + 64 + (4 * t + (lane >> 4)) * 32;
            const f32x4 u = *(const LAS f32x4*)(q), v = *(const LAS f32x4*)(q + 16);
            float o = wt_[0] * u[0];
            o = fmaf(wt_[1], u[1], o); o = fmaf(wt_[2], u[2], o); o = fmaf(wt_[3], u[3], o);
            o = fmaf(wt_[4], v[0], o); o = fmaf(wt_[5], v[1], o); o = fmaf(wt_[6], v[2], o); o = fmaf(wt_[7], v[3], o);
            o = rbf(o * inv) * sc[0];
            rows_mfma(acc, tl[4 + t], pk(o, o), mk0, mk1);
        }
        const int g2 = lane >> 4, c = lane & 15;
        const f32x4 a = (g2 & 2) ? acc[1] : acc[0];
        const float v0 = (g2 & 1) ? a[2] : a[0], v1 = (g2 & 1) ? a[3] : a[1];
        const int cons = 8 * wv + (c >> 1);
        st_sc1(rs, WS_X2 + cons * X2_STRIDE + w * 128 + ((c & 1) * 4 + g2) * 16, v4u{fu(v0), tag, fu(v1), tag});
    }
    STAMP(9);
    if ((w & 3) == 0) {
        const int xo = WS_X2 + (w >> 2) * X2_STRIDE + wv * 4096 + lane * 16;
        const unsigned xv = *(const GAS unsigned*)(p.x + 16 * (w >> 2) + 2 * (lane & 7));
        v4u d[4];
#pragma unroll
        for (int i = 0; i < 4; ++i) d[i] = ld_sc1(rs, xo + i * 1024);
        for (unsigned spins = 0;; ++spins) {
            unsigned pend = 0u;
#pragma unroll
            for (int i = 0; i < 4; ++i) if (!__all(d[i].y == tag && d[i].w == tag)) pend |= 1u << i;
            if (pend == 0u) break;
            if (spins > SPIN_CAP) { dead = true; break; }
            asm volatile("" ::: "memory");
#pragma unroll
            for (int i = 0; i < 4; ++i) if ((pend >> i) & 1u) d[i] = ld_sc1(rs, xo + i * 1024);
        }
        float s0 = (uf(d[0].x) + uf(d[1].x)) + (uf(d[2].x) + uf(d[3].x)), s1 = (uf(d[0].z) + uf(d[1].z)) + (uf(d[2].z) + uf(d[3].z));
        s0 += dpp<XOR8>(s0); s1 += dpp<XOR8>(s1);
        s0 = x16x32_sum(s0); s1 = x16x32_sum(s1);
        if (dead) s0 = __builtin_nanf("");
        if (lane < 8) *(LAS f32x2*)(L + L_X2 + wv * 64 + lane * 8) = f32x2{s0, s1};
        STAMP(10);
        WG_BARRIER();
        if (wv == 0 && lane < 8) {
            f32x2 s = {0.f, 0.f};
#pragma unroll
            for (int i = 0; i < NSW; ++i) s += *(const LAS f32x2*)(L + L_X2 + i * 64 + lane * 8);
            const int col = 16 * (w >> 2) + 2 * lane;
            *(GAS unsigned*)(p.out + col) = pk(fmaf(1.f - sc[0], bflo(xv), s[0]), fmaf(1.f - sc[0], bfhi(xv), s[1]));
            if (w == 0 && lane == 0) __builtin_amdgcn_raw_buffer_store_b32(tag, rs, WS_EPOCH, 0, AUX_SC1);
        }
        STAMP(11);
    }
    STAMP_FLUSH();
}

extern "C" void kernel_launch(void* const* d_in, const int* in_sizes, int n_in, void* d_out, int out_size, void* d_ws, size_t ws_size,
                              hipStream_t stream) {
    Params p;
    p.x = (const unsigned short*)d_in[0];
    p.kc = (const unsigned short*)d_in[1];
    p.vc = (const unsigned short*)d_in[2];
    p.wq = (const unsigned short*)d_in[3];
    p.wo = (const unsigned short*)d_in[4];
    p.wr = (const unsigned short*)d_in[6];
    p.wg = (const unsigned short*)d_in[7];
    p.wu = (const unsigned short*)d_in[8];
    p.wd = (const unsigned short*)d_in[9];
    p.gw = (const unsigned short*)d_in[10];
    p.out = (unsigned short*)d_out;
    p.ws = (unsigned char*)d_ws;
    (void)hipFuncSetAttribute((const void*)fused_decode_kernel, hipFuncAttributeMaxDynamicSharedMemorySize, LDS_BYTES);
    hipLaunchKernelGGL(fused_decode_kernel, dim3(NWG), dim3(NTHR), LDS_BYTES, stream, p);
}
```

```cpp
#include <hip/hip_runtime.h>
#include <hip/hip_bf16.h>
#include <stdint.h>

#define GAS __attribute__((address_space(1)))
#define LAS __attribute__((address_space(3)))

typedef unsigned v4u __attribute__((ext_vector_type(4)));
typedef unsigned v2u __attribute__((ext_vector_type(2)));
typedef float f32x4 __attribute__((ext_vector_type(4)));
typedef float f32x2 __attribute__((ext_vector_type(2)));
typedef short bf16x8 __attribute__((ext_vector_type(8)));
typedef short bf16x4 __attribute__((ext_vector_type(4)));
typedef __bf16 bf2 __attribute__((ext_vector_type(2)));

namespace {

constexpr int NWG = 256, NSW = 8, NTHR = (NSW + 1) * 64;
constexpr int DM = 1024, SK = 65536, DH = 128, FF = 3584, NQ = 4096;
#define EARLY 2
#define TAILD 4
#define TAILDM1 3
#define X1FEW 8
#define X1SLEEP 0
#define TLATE 8
#define DEPTH 4
#define DTW 28
#define PROG1 4
#define PROG2 13
constexpr int TPR = 4, NROUND = 8;
constexpr int E_KV = 64, E_END = E_KV + 8 - TLATE;
constexpr int DT = DTW;
constexpr int jmax(int e) {
    const int a = DT - 1 + (e + 2) / 2, b = e + DEPTH + 1;
    const int m = a > b ? a : b;
    return m < E_END - 1 ? m : E_END - 1;
}

constexpr int WS_EPOCH = 0;
constexpr int WS_Q = 4096;
constexpr int WS_Q1 = WS_Q + 2048 * 8;
constexpr int Q1_STRIDE = 32 * 128;
constexpr int WS_ML = WS_Q1 + NWG * Q1_STRIDE;
constexpr int WS_X1 = WS_ML + 32 * 64 * 16;
constexpr int X1_STRIDE = 64 * 128;
constexpr int WS_X2 = WS_X1 + NWG * X1_STRIDE;
constexpr int X2_STRIDE = NWG * 128;
constexpr int WS_H = WS_X2 + 64 * X2_STRIDE;
constexpr int H_STRIDE = 32 * 256;
constexpr int WS_END = WS_H + NWG * H_STRIDE;

constexpr int L_FLAGS = 0;
constexpr int L_HV = 128;
constexpr int L_SC = 192;
constexpr int L_QF = 256;
constexpr int L_ML = L_QF + 2048;
constexpr int L_X1 = L_ML + 512;
constexpr int L_X2 = L_X1 + 64 + 512;
constexpr int L_GU = L_X2 + 512;
constexpr int L_QP = L_GU + 8 * 2 * 448 * 4;
constexpr int L_MRG = L_QP + 8 * 512 * 4;
constexpr int L_END = L_MRG + 8 * 8 * 128 * 4;
constexpr int LDS_BYTES = 88 * 1024;
static_assert(L_END <= LDS_BYTES, "lds");
static_assert(LDS_BYTES <= 160 * 1024 && LDS_BYTES > 80 * 1024, "lds");
static_assert(L_MRG % 16 == 0 && L_GU % 16 == 0 && L_QP % 16 == 0, "align");

constexpr unsigned SPIN_CAP = 1u << 18;
constexpr int AUX_SC1 = 16;

struct Params {
    const unsigned short* x;
    const unsigned short* kc;
    const unsigned short* vc;
    const unsigned short* wq;
    const unsigned short* wo;
    const unsigned short* wr;
    const unsigned short* wg;
    const unsigned short* wu;
    const unsigned short* wd;
    const unsigned short* gw;
    unsigned short* out;
    unsigned char* ws;
};

__device__ __forceinline__ v4u ldnt(const unsigned char* p) { return __builtin_nontemporal_load((const GAS v4u*)p); }
__device__ __forceinline__ float bflo(unsigned u) { return __builtin_bit_cast(float, u << 16); }
__device__ __forceinline__ float bfhi(unsigned u) { return __builtin_bit_cast(float, u & 0xffff0000u); }
__device__ __forceinline__ unsigned pk(float lo, float hi) {
    bf2 r = {(__bf16)lo, (__bf16)hi};
    return __builtin_bit_cast(unsigned, r);
}
__device__ __forceinline__ float rbf(float v) { return bflo(pk(v, v)); }
__device__ __forceinline__ unsigned fu(float f) { return __builtin_bit_cast(unsigned, f); }
__device__ __forceinline__ float uf(unsigned u) { return __builtin_bit_cast(float, u); }
template <int CTRL> __device__ __forceinline__ float dpp(float x) {
    return uf((unsigned)__builtin_amdgcn_mov_dpp((int)fu(x), CTRL, 0xf, 0xf, true));
}
constexpr int XOR1 = 0xB1, XOR2 = 0x4E, XOR7 = 0x141, XOR8 = 0x128, ROR4 = 0x124;
__device__ __forceinline__ float swap32_add(float a, float b) {
    auto s = __builtin_amdgcn_permlane32_swap(fu(a), fu(b), false, false);
    return uf(s[0]) + uf(s[1]);
}
__device__ __forceinline__ float swap16_add(float a, float b) {
    auto s = __builtin_amdgcn_permlane16_swap(fu(a), fu(b), false, false);
    return uf(s[0]) + uf(s[1]);
}
__device__ __forceinline__ float x16x32_max(float x) {
    auto s = __builtin_amdgcn_permlane16_swap(fu(x), fu(x), false, false);
    x = fmaxf(uf(s[0]), uf(s[1]));
    auto t = __builtin_amdgcn_permlane32_swap(fu(x), fu(x), false, false);
    return fmaxf(uf(t[0]), uf(t[1]));
}
__device__ __forceinline__ float x16x32_sum(float x) { x = swap16_add(x, x); return swap32_add(x, x); }
__device__ __forceinline__ float row_sum(float x) {
    x += dpp<XOR1>(x); x += dpp<XOR2>(x); x += dpp<XOR7>(x); x += dpp<XOR8>(x); return x;
}
__device__ __forceinline__ float row_max(float x) {
    x = fmaxf(x, dpp<XOR1>(x)); x = fmaxf(x, dpp<XOR2>(x)); x = fmaxf(x, dpp<XOR7>(x)); x = fmaxf(x, dpp<XOR8>(x)); return x;
}
__device__ __forceinline__ float wave_sum(float x) { return x16x32_sum(row_sum(x)); }
__device__ __forceinline__ float wave_max(float x) { return x16x32_max(row_max(x)); }

__device__ __forceinline__ f32x4 mfma32(v4u a, v4u b, f32x4 c) {
    return __builtin_amdgcn_mfma_f32_16x16x32_bf16(__builtin_bit_cast(bf16x8, a), __builtin_bit_cast(bf16x8, b), c, 0, 0, 0);
}
__device__ __forceinline__ f32x4 mfma16(v2u a, v2u b, f32x4 c) {
    return __builtin_amdgcn_mfma_f32_16x16x16bf16_1k(__builtin_bit_cast(bf16x4, a), __builtin_bit_cast(bf16x4, b), c, 0, 0, 0);
}
#define WG_BARRIER() do { asm volatile("s_waitcnt lgkmcnt(0)" ::: "memory"); __builtin_amdgcn_s_barrier(); asm volatile("" ::: "memory"); } while (0)
#define SB() __builtin_amdgcn_sched_barrier(0)
#define SBMASK 0x10F
#define SBM() __builtin_amdgcn_sched_barrier(SBMASK)
#define STR2(x) #x
#define STR(x) STR2(x)
#define THROTTLE() asm volatile("s_waitcnt vmcnt(" STR(DEPTHM1) ")" ::: "memory")
#define DEPTHM1 3

__device__ __forceinline__ void fma8(float (&acc)[8], v4u wv, float xv) {
    acc[0] = fmaf(xv, bflo(wv.x), acc[0]); acc[1] = fmaf(xv, bfhi(wv.x), acc[1]);
    acc[2] = fmaf(xv, bflo(wv.y), acc[2]); acc[3] = fmaf(xv, bfhi(wv.y), acc[3]);
    acc[4] = fmaf(xv, bflo(wv.z), acc[4]); acc[5] = fmaf(xv, bfhi(wv.z), acc[5]);
    acc[6] = fmaf(xv, bflo(wv.w), acc[6]); acc[7] = fmaf(xv, bfhi(wv.w), acc[7]);
}
__device__ __forceinline__ f32x2 reduce_cols(const float (&a)[8]) {
    const float b0 = swap32_add(a[0], a[4]), b1 = swap32_add(a[1], a[5]), b2 = swap32_add(a[2], a[6]), b3 = swap32_add(a[3], a[7]);
    float c0 = swap16_add(b0, b2), c1 = swap16_add(b1, b3);
    c0 += dpp<XOR2>(c0); c1 += dpp<XOR2>(c1);
    c0 += dpp<ROR4>(c0); c1 += dpp<ROR4>(c1);
    c0 += dpp<XOR8>(c0); c1 += dpp<XOR8>(c1);
    return f32x2{c0, c1};
}

__device__ __forceinline__ v4u ld_sc1(__amdgpu_buffer_rsrc_t rs, int off) { return __builtin_amdgcn_raw_buffer_load_b128(rs, off, 0, AUX_SC1); }
__device__ __forceinline__ void st_sc1(__amdgpu_buffer_rsrc_t rs, int off, v4u v) { __builtin_amdgcn_raw_buffer_store_b128(v, rs, off, 0, AUX_SC1); }

__device__ __forceinline__ void rows_mfma(f32x4 (&acc)[2], v4u wv, unsigned pp, unsigned m0, unsigned m1) {
    const v2u a = {pp & m0, pp & m1};
    acc[0] = mfma16(a, v2u{wv.x, wv.y}, acc[0]);
    acc[1] = mfma16(a, v2u{wv.z, wv.w}, acc[1]);
}
__device__ __forceinline__ unsigned lds_arrive(LAS unsigned char* L, int word, int lane) {
    asm volatile("s_waitcnt lgkmcnt(0)" ::: "memory");
    unsigned old = 0;
    if (lane == 0) old = __hip_atomic_fetch_add((LAS unsigned*)(L + L_FLAGS + 4 * word), 1u, __ATOMIC_RELAXED, __HIP_MEMORY_SCOPE_WORKGROUP);
    old = __builtin_amdgcn_readfirstlane(old);
    asm volatile("" ::: "memory");
    return old;
}
__device__ __forceinline__ void lds_wait_slow(volatile LAS unsigned* flags, int word, unsigned want) {
    for (unsigned spins = 0; flags[word] < want && spins < SPIN_CAP; ++spins) __builtin_amdgcn_s_sleep(8);
    asm volatile("" ::: "memory");
}
__device__ __forceinline__ void lds_wait(volatile LAS unsigned* flags, int word, unsigned want) {
    for (unsigned spins = 0; flags[word] < want && spins < (SPIN_CAP << 4); ++spins) __builtin_amdgcn_s_sleep(1);
    asm volatile("" ::: "memory");
}
__device__ __forceinline__ float qred(f32x4 d) {
    float t = dpp<0x00>(d[0]);
    t = dpp<0x55>(d[1]) + t;
    asm volatile("" : "+v"(t));
    t = dpp<0xAA>(d[2]) + t;
    t = dpp<0xFF>(d[3]) + t;
    return t;
}
template <int T> __device__ __forceinline__ unsigned qbc(unsigned x) {
    return (unsigned)__builtin_amdgcn_mov_dpp((int)x, T * 0x55, 0xf, 0xf, true);
}

#define STAMP(i) do {} while (0)
#define STAMP_DECL() do {} while (0)
#define STAMP_FLUSH() do {} while (0)

}

__global__ void __launch_bounds__(NTHR, 1) fused_decode_kernel(Params p) {
    extern __shared__ __attribute__((aligned(16))) unsigned char smem[];
    LAS unsigned char* L = (LAS unsigned char*)smem;
    volatile LAS unsigned* flags = (volatile LAS unsigned*)(L + L_FLAGS);
    const int tid = threadIdx.x, lane = tid & 63;
    const int wv = __builtin_amdgcn_readfirstlane(tid >> 6);
    const int b = blockIdx.x;
    const int w = (b & 7) * 32 + (b >> 3);
    const int kvh = w >> 6, sp = w & 63;
    const int cb = w & 7, rb = w >> 3;
    const __amdgpu_buffer_rsrc_t rs = __builtin_amdgcn_make_buffer_rsrc((void*)p.ws, 0, WS_END, 0x00020000);
    STAMP_DECL();
    STAMP(0);

    if (wv == NSW) {
        const unsigned ep = __builtin_amdgcn_raw_buffer_load_b32(rs, WS_EPOCH, 0, AUX_SC1);
        if (lane < 8) flags[lane] = 0u;
        WG_BARRIER();
        const unsigned tag = __builtin_amdgcn_readfirstlane(ep) + 1u;
        if (lane == 0) flags[3] = tag;
        bool dead = false;
        STAMP(1);
        lds_wait(flags, 1, NSW);
#pragma unroll
        for (int it = 0; it < 4; ++it) {
            const int c = it * 128 + lane * 2;
            f32x2 s = {0.f, 0.f};
#pragma unroll
            for (int i = 0; i < NSW; ++i) s += *(const LAS f32x2*)(L + L_QP + (i * 512 + c) * 4);
            st_sc1(rs, WS_Q1 + (cb * 32 + (c >> 4)) * Q1_STRIDE + rb * 128 + (c & 15) * 8, v4u{fu(s[0]), tag, fu(s[1]), tag});
        }
        STAMP(2);
        lds_wait_slow(flags, 4, 1u);
        {
            const int xo = WS_Q1 + w * Q1_STRIDE;
            v4u d[4];
            for (unsigned spins = 0;; ++spins) {
                asm volatile("" ::: "memory");
#pragma unroll
                for (int i = 0; i < 4; ++i) d[i] = ld_sc1(rs, xo + i * 1024 + lane * 16);
                bool ok = true;
#pragma unroll
                for (int i = 0; i < 4; ++i) ok = ok && d[i].y == tag && d[i].w == tag;
                if (__all(ok)) break;
                if (spins > SPIN_CAP) { dead = true; break; }
                __builtin_amdgcn_s_sleep(8);
            }
            float s0 = (uf(d[0].x) + uf(d[1].x)) + (uf(d[2].x) + uf(d[3].x)), s1 = (uf(d[0].z) + uf(d[1].z)) + (uf(d[2].z) + uf(d[3].z));
            s0 += dpp<XOR8>(s0); s1 += dpp<XOR8>(s1);
            s0 = x16x32_sum(s0); s1 = x16x32_sum(s1);
            const unsigned pr = pk(s0, s1);
            const unsigned nx = __builtin_amdgcn_mov_dpp((int)pr, XOR1, 0xf, 0xf, true);
            if (lane < 8 && !(lane & 1) && !dead) st_sc1(rs, WS_Q + (w * 8 + lane) * 8, v4u{pr, tag, nx, tag});
        }
        STAMP(3);
        lds_wait(flags, 2, NSW);
#pragma unroll
        for (int it = 0; it < 7; ++it) {
            const int idx = it * 64 + lane, j = idx / 14, k = idx - 14 * j;
            const int mat = k >= 7 ? 1 : 0, u = 14 * j + 2 * (k - 7 * mat);
            f32x2 s = {0.f, 0.f};
#pragma unroll
            for (int i = 0; i < NSW; ++i) s += *(const LAS f32x2*)(L + L_GU + ((i * 2 + mat) * 448 + u) * 4);
            st_sc1(rs, WS_H + (cb * 32 + j) * H_STRIDE + rb * 256 + k * 16, v4u{fu(s[0]), tag, fu(s[1]), tag});
        }
        lds_wait_slow(flags, 5, 1u);
        {
            const int qoff = WS_Q + (kvh * 512 + ((lane >> 2) & 3) * 64 + (4 * (lane >> 4) + (lane & 3)) * 4) * 8;
            v4u g0, g1, g2, g3;
            for (unsigned spins = 0;; ++spins) {
                asm volatile("" ::: "memory");
                g0 = ld_sc1(rs, qoff); g1 = ld_sc1(rs, qoff + 16); g2 = ld_sc1(rs, qoff + 2048); g3 = ld_sc1(rs, qoff + 2048 + 16);
                const bool ok = g0.y == tag && g0.w == tag && g1.y == tag && g1.w == tag && g2.y == tag && g2.w == tag && g3.y == tag && g3.w == tag;
                if (__all(ok) || dead) break;
                if (spins > SPIN_CAP) { dead = true; break; }
                __builtin_amdgcn_s_sleep(4);
            }
            *(LAS v4u*)(L + L_QF + lane * 16) = v4u{g0.x, g0.z, g1.x, g1.z};
            *(LAS v4u*)(L + L_QF + 1024 + lane * 16) = v4u{g2.x, g2.z, g3.x, g3.z};
            asm volatile("s_waitcnt lgkmcnt(0)" ::: "memory");
            if (lane == 0) flags[0] = dead ? 2u : 1u;
        }
        STAMP(4);
        {
            const GAS v4u* xp = (const GAS v4u*)p.x; const GAS v4u* gp = (const GAS v4u*)p.gw; const GAS v4u* rp = (const GAS v4u*)p.wr;
            float sg = 0.f, sr = 0.f;
#pragma unroll
            for (int i = 0; i < 2; ++i) {
                const v4u xv = xp[lane + 64 * i], gv = gp[lane + 64 * i], rv = rp[lane + 64 * i];
#pragma unroll
                for (int k = 0; k < 4; ++k) {
                    sg += rbf(bflo(xv[k]) * bflo(gv[k])); sg += rbf(bfhi(xv[k]) * bfhi(gv[k]));
                    sr = fmaf(bflo(xv[k]), bflo(rv[k]), sr); sr = fmaf(bfhi(xv[k]), bfhi(rv[k]), sr);
                }
            }
            sg = rbf(wave_sum(sg)); sr = rbf(wave_sum(sr));
            const float g_gate = rbf(1.f / (1.f + __expf(-sg)));
            const float rsg = rbf(1.f / (1.f + __expf(-sr)));
            if (lane == 0) *(LAS f32x2*)(L + L_SC) = f32x2{g_gate, (1.f - g_gate) * rsg};
        }
        lds_wait_slow(flags, 6, 1u);
        {
            const int xo = WS_H + w * H_STRIDE;
            const bool used = (lane & 15) < 14;
            v4u d[8];
            for (unsigned spins = 0;; ++spins) {
                asm volatile("" ::: "memory");
#pragma unroll
                for (int i = 0; i < 8; ++i) d[i] = ld_sc1(rs, xo + i * 1024 + lane * 16);
                bool ok = true;
#pragma unroll
                for (int i = 0; i < 8; ++i) ok = ok && d[i].y == tag && d[i].w == tag;
                if (__all(ok || !used) || dead) break;
                if (spins > SPIN_CAP) { dead = true; break; }
                __builtin_amdgcn_s_sleep(32);
            }
            float s0 = 0.f, s1 = 0.f;
#pragma unroll
            for (int i = 0; i < 8; ++i) { s0 += uf(d[i].x); s1 += uf(d[i].z); }
            s0 = rbf(x16x32_sum(s0)); s1 = rbf(x16x32_sum(s1));
            const float u0 = __shfl(s0, (lane & 15) + 7), u1 = __shfl(s1, (lane & 15) + 7);
            const float a0 = rbf(s0 / (1.f + __expf(-s0))), a1 = rbf(s1 / (1.f + __expf(-s1)));
            float h0 = rbf(a0 * u0), h1 = rbf(a1 * u1);
            if (lane >= 7) { h0 = 0.f; h1 = 0.f; }
            if (dead) h0 = __builtin_nanf("");
            if (lane < 8) *(LAS f32x2*)(L + L_HV + lane * 8) = f32x2{h0, h1};
        }
        STAMP(5);
        WG_BARRIER();
        STAMP_FLUSH();
        return;
    }

    const int grow = 32 * rb + 4 * wv;
    const unsigned char* wq_p = (const unsigned char*)p.wq + (size_t)grow * (NQ * 2) + cb * 1024 + lane * 16;
    const size_t kv_base = ((size_t)kvh * SK + (size_t)sp * 1024 + wv * 4) * 256;
    const unsigned char* k_p = (const unsigned char*)p.kc + kv_base + ((lane >> 2) & 3) * 256 + (4 * (lane >> 4) + (lane & 3)) * 16;
    const unsigned char* v_p = (const unsigned char*)p.vc + kv_base + lane * 16;
    const int glane = lane < 56 ? lane : 55;
    const size_t goff = (size_t)grow * (FF * 2) + cb * 896 + glane * 16;
    const unsigned char* wg_p = (const unsigned char*)p.wg + goff;
    const unsigned char* wu_p = (const unsigned char*)p.wu + goff;
    const unsigned char* wd_p = (const unsigned char*)p.wd + (size_t)(w * 14) * (DM * 2) + wv * 256 + (lane & 15) * 16;
    const unsigned char* wo_p = (const unsigned char*)p.wo + (size_t)(w * 16 + (lane >> 4)) * (DM * 2) + wv * 256 + (lane & 15) * 16;

    auto issue_w = [&](int i) -> v4u {
        if (i < 4) return ldnt(wq_p + (size_t)i * (NQ * 2));
        if (i < 8) return ldnt(wg_p + (size_t)(i - 4) * (FF * 2));
        if (i < 12) return ldnt(wu_p + (size_t)(i - 8) * (FF * 2));
        if (i < 16) { const int r = 4 * (i - 12) + (lane >> 4); return ldnt(wd_p + (size_t)(r < 14 ? r : 13) * (DM * 2)); }
        return ldnt(wo_p + (size_t)(i - 16) * 4 * (DM * 2));
    };
    auto issue = [&](int e) -> v4u {
        const int r = e / (2 * TPR), k = e % (2 * TPR);
        return k < TPR ? ldnt(k_p + (size_t)(r * TPR + k) * 8192) : ldnt(v_p + (size_t)(r * TPR + k - TPR) * 8192);
    };
    v4u ring[DT], tl[8];
#define ISSUE(j) do { THROTTLE(); if ((j) < E_KV) ring[(j) % DT] = issue(j); else tl[(j) - E_KV < 0 ? 0 : (j) - E_KV] = issue_w(12 + (j) - E_KV); SB(); } while (0)
#define AFTER(e) do { if (jmax(e) > ((e) == 0 ? DT - 1 : jmax((e) - 1))) ISSUE(jmax(e)); } while (0)
#define GET(e) ring[(e) % DT]

    const v2u xg = *(const GAS v2u*)(p.x + grow);
    v4u wt[12];
#pragma unroll
    for (int i = 0; i < 4; ++i) wt[i] = issue_w(i);
    SB();
    WG_BARRIER();
#pragma unroll
    for (int i = 4; i < 4 + EARLY; ++i) wt[i] = issue_w(i);
    SB();
    bool dead = false;
    {
        const float xv[4] = {bflo(xg.x), bfhi(xg.x), bflo(xg.y), bfhi(xg.y)};
        float acc[8] = {0.f, 0.f, 0.f, 0.f, 0.f, 0.f, 0.f, 0.f};
#pragma unroll
        for (int t = 0; t < 4; ++t) fma8(acc, wt[t], xv[t]);
        {
            LAS unsigned char* q = L + L_QP + (wv * 512 + lane * 8) * 4;
            *(LAS f32x4*)(q) = f32x4{acc[0], acc[1], acc[2], acc[3]}; *(LAS f32x4*)(q + 16) = f32x4{acc[4], acc[5], acc[6], acc[7]};
        }
        lds_arrive(L, 1, lane);
        STAMP(1);
        float ag[8] = {0.f, 0.f, 0.f, 0.f, 0.f, 0.f, 0.f, 0.f}, au[8] = {0.f, 0.f, 0.f, 0.f, 0.f, 0.f, 0.f, 0.f};
#pragma unroll
        for (int t = 4 + EARLY; t < 12; ++t) { THROTTLE(); wt[t] = issue_w(t); SB(); }
#pragma unroll
        for (int j = 0; j < 4; ++j) ISSUE(j);
#pragma unroll
        for (int t = 0; t < 4; ++t) { fma8(ag, wt[4 + t], xv[t]); }
#pragma unroll
        for (int t = 0; t < 4; ++t) { fma8(au, wt[8 + t], xv[t]); }
        if (lane < 56) {
            LAS unsigned char* q = L + L_GU + (wv * 2 * 448 + lane * 8) * 4;
            *(LAS f32x4*)(q) = f32x4{ag[0], ag[1], ag[2], ag[3]}; *(LAS f32x4*)(q + 16) = f32x4{ag[4], ag[5], ag[6], ag[7]};
            *(LAS f32x4*)(q + 1792) = f32x4{au[0], au[1], au[2], au[3]}; *(LAS f32x4*)(q + 1792 + 16) = f32x4{au[4], au[5], au[6], au[7]};
        }
        lds_arrive(L, 2, lane);
    }
#pragma unroll
    for (int j = 4; j < DT; ++j) {
        ISSUE(j);
        if (j == PROG1 && wv == 0 && lane == 0) flags[4] = 1u;
        if (j == PROG2 && wv == 0 && lane == 0) flags[5] = 1u;
    }
    STAMP(2);
    lds_wait(flags, 0, 1u);
    if (flags[0] != 1u) dead = true;
    const v4u qf0 = *(const LAS v4u*)(L + L_QF + lane * 16), qf1 = *(const LAS v4u*)(L + L_QF + 1024 + lane * 16);
    STAMP(3);

    constexpr float C2 = 0.088388347648318440f * 1.4426950408889634f;
    const unsigned mk0 = (lane & 3) == 0 ? 0x0000ffffu : (lane & 3) == 1 ? 0xffff0000u : 0u;
    const unsigned mk1 = (lane & 3) == 2 ? 0x0000ffffu : (lane & 3) == 3 ? 0xffff0000u : 0u;
    const int sub = lane & 3;
    f32x4 o00 = {0.f, 0.f, 0.f, 0.f}, o01 = o00, o10 = o00, o11 = o00;
    float m0 = 0.f, m1 = 0.f, l0 = 0.f, l1 = 0.f;
#pragma unroll
    for (int r = 0; r < NROUND; ++r) {
        const int base = r * 2 * TPR;
        const f32x4 z = {0.f, 0.f, 0.f, 0.f};
        f32x4 d0[TPR], d1[TPR];
        float P0, P1;
        d0[0] = mfma32(GET(base + 0), qf0, z); d1[0] = mfma32(GET(base + 0), qf1, z); AFTER(base + 0);
        d0[1] = mfma32(GET(base + 1), qf0, z); d1[1] = mfma32(GET(base + 1), qf1, z); AFTER(base + 1);
        d0[2] = mfma32(GET(base + 2), qf0, z); d1[2] = mfma32(GET(base + 2), qf1, z);
        P0 = qred(d0[0]); P1 = qred(d1[0]);
        AFTER(base + 2);
        d0[3] = mfma32(GET(base + 3), qf0, z); d1[3] = mfma32(GET(base + 3), qf1, z);
        { const float a = qred(d0[1]), c = qred(d1[1]); P0 = sub == 1 ? a : P0; P1 = sub == 1 ? c : P1; }
        AFTER(base + 3);
        { const float a = qred(d0[2]), c = qred(d1[2]); P0 = sub == 2 ? a : P0; P1 = sub == 2 ? c : P1; }
        { const float a = qred(d0[3]), c = qred(d1[3]); P0 = sub == 3 ? a : P0; P1 = sub == 3 ? c : P1; }
        float t0 = fmaxf(P0, dpp<XOR1>(P0)), t1 = fmaxf(P1, dpp<XOR1>(P1));
        t0 = fmaxf(t0, dpp<XOR2>(t0)); t1 = fmaxf(t1, dpp<XOR2>(t1));
        t0 = x16x32_max(t0) * C2; t1 = x16x32_max(t1) * C2;
        if (r == 0) { m0 = t0; m1 = t1; }
        else if (__any(t0 > m0 || t1 > m1)) {
            const float n0 = fmaxf(m0, t0), n1 = fmaxf(m1, t1);
            const float a0 = __builtin_amdgcn_exp2f(m0 - n0), a1 = __builtin_amdgcn_exp2f(m1 - n1);
            m0 = n0; m1 = n1; l0 *= a0; l1 *= a1;
            const float b0 = __shfl(a0, (lane >> 4) * 20), b1 = __shfl(a1, (lane >> 4) * 20);
            o00 *= b0; o01 *= b0; o10 *= b1; o11 *= b1;
        }
        const float p0 = __builtin_amdgcn_exp2f(fmaf(P0, C2, -m0)), p1 = __builtin_amdgcn_exp2f(fmaf(P1, C2, -m1));
        l0 += p0; l1 += p1;
        const unsigned pp0 = pk(p0, p0), pp1 = pk(p1, p1);
#define PV(t) do { \
            const unsigned b0_ = qbc<t>(pp0), b1_ = qbc<t>(pp1); \
            const v2u a0_ = {b0_ & mk0, b0_ & mk1}, a1_ = {b1_ & mk0, b1_ & mk1}; \
            const v4u vf_ = GET(base + TPR + (t)); \
            o00 = mfma16(a0_, v2u{vf_.x, vf_.y}, o00); o01 = mfma16(a0_, v2u{vf_.z, vf_.w}, o01); \
            o10 = mfma16(a1_, v2u{vf_.x, vf_.y}, o10); o11 = mfma16(a1_, v2u{vf_.z, vf_.w}, o11); \
            AFTER(base + TPR + (t)); } while (0)
        PV(0); PV(1); PV(2); PV(3);
        if (r == NROUND / 2 - 1) { STAMP(4); if (wv == 0 && lane == 0) flags[6] = 1u; }
    }
#pragma unroll
    for (int j = jmax(E_KV - 1) + 1; j < E_END; ++j) tl[j - E_KV] = issue_w(12 + j - E_KV);
    SB();
    STAMP(5);
    l0 += dpp<XOR1>(l0); l1 += dpp<XOR1>(l1); l0 += dpp<XOR2>(l0); l1 += dpp<XOR2>(l1);
    l0 = x16x32_sum(l0); l1 = x16x32_sum(l1);
    {
        LAS unsigned char* mb = L + L_MRG + wv * 4096 + (lane >> 4) * 512 + (lane & 15) * 32;
        *(LAS f32x4*)(mb) = o00; *(LAS f32x4*)(mb + 16) = o01;
        *(LAS f32x4*)(mb + 2048) = o10; *(LAS f32x4*)(mb + 2048 + 16) = o11;
        if ((lane & 0x33) == 0) {
            LAS unsigned char* q = L + L_ML + wv * 64 + (lane >> 2) * 8;
            *(LAS f32x2*)(q) = f32x2{m0, dead ? __builtin_nanf("") : l0}; *(LAS f32x2*)(q + 32) = f32x2{m1, l1};
        }
    }
    WG_BARRIER();
    STAMP(6);
    const unsigned tag = flags[3];
    {
        f32x2 ml[NSW];
#pragma unroll
        for (int i = 0; i < NSW; ++i) ml[i] = *(const LAS f32x2*)(L + L_ML + i * 64 + wv * 8);
        float M = ml[0][0];
#pragma unroll
        for (int i = 1; i < NSW; ++i) M = fmaxf(M, ml[i][0]);
        float Ls = 0.f, a0 = 0.f, a1 = 0.f;
#pragma unroll
        for (int i = 0; i < NSW; ++i) {
            const float wt_ = __builtin_amdgcn_exp2f(ml[i][0] - M);
            const f32x2 o = *(const LAS f32x2*)(L + L_MRG + i * 4096 + wv * 512 + lane * 8);
            Ls = fmaf(wt_, ml[i][1], Ls); a0 = fmaf(wt_, o[0], a0); a1 = fmaf(wt_, o[1], a1);
        }
        st_sc1(rs, WS_X1 + (kvh * 64 + wv * 8 + (lane >> 3)) * X1_STRIDE + sp * 128 + (lane & 7) * 16, v4u{fu(a0), tag, fu(a1), tag});
        if (lane == 0) st_sc1(rs, WS_ML + ((kvh * 8 + wv) * 64 + sp) * 16, v4u{fu(M), tag, fu(Ls), tag});
    }
    STAMP(7);
    const f32x2 sc = *(const LAS f32x2*)(L + L_SC);
    f32x4 acc[2] = {{0.f, 0.f, 0.f, 0.f}, {0.f, 0.f, 0.f, 0.f}};
    {
        const int xo = WS_X1 + w * X1_STRIDE + wv * 1024 + lane * 16;
        const int mo = WS_ML + ((w >> 3) * 64 + wv * 8 + (lane >> 3)) * 16;
        v4u ml = ld_sc1(rs, mo), d = ld_sc1(rs, xo);
        SB();
#pragma unroll
        for (int t = 8 - TLATE; t < 8; ++t) { tl[t] = issue_w(12 + t); }
        SB();
        {
            unsigned long long got = __ballot(ml.y == tag && ml.w == tag);
            bool have = got == ~0ull && __all(d.y == tag && d.w == tag);
            for (unsigned spins = 0; !have; ++spins) {
                if (X1SLEEP) __builtin_amdgcn_s_sleep(X1SLEEP);
                asm volatile("" ::: "memory");
                if (got != ~0ull) ml = ld_sc1(rs, mo);
                if (__builtin_popcountll(~got) <= X1FEW) d = ld_sc1(rs, xo);
                got = __ballot(ml.y == tag && ml.w == tag);
                have = got == ~0ull && __all(d.y == tag && d.w == tag);
                if (spins > SPIN_CAP) { dead = true; break; }
            }
        }
        const float m = uf(ml.x);
        float M = fmaxf(m, dpp<XOR8>(m)); M = x16x32_max(M);
        const float wt_ = __builtin_amdgcn_exp2f(m - M);
        float Ls = wt_ * uf(ml.z), o0 = wt_ * uf(d.x), o1 = wt_ * uf(d.z);
        Ls += dpp<XOR8>(Ls); o0 += dpp<XOR8>(o0); o1 += dpp<XOR8>(o1);
        Ls = x16x32_sum(Ls); o0 = x16x32_sum(o0); o1 = x16x32_sum(o1);
        if (lane < 8) {
            *(LAS float*)(L + L_X1 + 64 + ((2 * lane) * 8 + wv) * 4) = o0;
            *(LAS float*)(L + L_X1 + 64 + ((2 * lane + 1) * 8 + wv) * 4) = o1;
            if (lane == 0) *(LAS f32x2*)(L + L_X1 + wv * 8) = f32x2{M, dead ? __builtin_nanf("") : Ls};
        }
#pragma unroll
        for (int t = 0; t < 4; ++t) {
            const float h = *(const LAS float*)(L + L_HV + (4 * t + (lane >> 4)) * 4) * sc[1];
            rows_mfma(acc, tl[t], pk(h, h), mk0, mk1);
        }
    }
    STAMP(8);
    WG_BARRIER();
    {
        f32x2 ml[NSW];
#pragma unroll
        for (int i = 0; i < NSW; ++i) ml[i] = *(const LAS f32x2*)(L + L_X1 + i * 8);
        float M = ml[0][0];
#pragma unroll
        for (int i = 1; i < NSW; ++i) M = fmaxf(M, ml[i][0]);
        float wt_[NSW], Ls = 0.f;
#pragma unroll
        for (int i = 0; i < NSW; ++i) { wt_[i] = __builtin_amdgcn_exp2f(ml[i][0] - M); Ls = fmaf(wt_[i], ml[i][1], Ls); }
        const float inv = 1.f / Ls;
#pragma unroll
        for (int t = 0; t < 4; ++t) {
            const LAS unsigned char* q = L + L_X1 + 64 + (4 * t + (lane >> 4)) * 32;
            const f32x4 u = *(const LAS f32x4*)(q), v = *(const LAS f32x4*)(q + 16);
            float o = wt_[0] * u[0];
            o = fmaf(wt_[1], u[1], o); o = fmaf(wt_[2], u[2], o); o = fmaf(wt_[3], u[3], o);
            o = fmaf(wt_[4], v[0], o); o = fmaf(wt_[5], v[1], o); o = fmaf(wt_[6], v[2], o); o = fmaf(wt_[7], v[3], o);
            o = rbf(o * inv) * sc[0];
            rows_mfma(acc, tl[4 + t], pk(o, o), mk0, mk1);
        }
        const int g2 = lane >> 4, c = lane & 15;
        const f32x4 a = (g2 & 2) ? acc[1] : acc[0];
        const float v0 = (g2 & 1) ? a[2] : a[0], v1 = (g2 & 1) ? a[3] : a[1];
        const int cons = 8 * wv + (c >> 1);
        st_sc1(rs, WS_X2 + cons * X2_STRIDE + w * 128 + ((c & 1) * 4 + g2) * 16, v4u{fu(v0), tag, fu(v1), tag});
    }
    STAMP(9);
    if ((w & 3) == 0) {
        const int xo = WS_X2 + (w >> 2) * X2_STRIDE + wv * 4096 + lane * 16;
        const unsigned xv = *(const GAS unsigned*)(p.x + 16 * (w >> 2) + 2 * (lane & 7));
        v4u d[4];
#pragma unroll
        for (int i = 0; i < 4; ++i) d[i] = ld_sc1(rs, xo + i * 1024);
        for (unsigned spins = 0;; ++spins) {
            unsigned pend = 0u;
#pragma unroll
            for (int i = 0; i < 4; ++i) if (!__all(d[i].y == tag && d[i].w == tag)) pend |= 1u << i;
            if (pend == 0u) break;
            if (spins > SPIN_CAP) { dead = true; break; }
            asm volatile("" ::: "memory");
#pragma unroll
            for (int i = 0; i < 4; ++i) if ((pend >> i) & 1u) d[i] = ld_sc1(rs, xo + i * 1024);
        }
        float s0 = (uf(d[0].x) + uf(d[1].x)) + (uf(d[2].x) + uf(d[3].x)), s1 = (uf(d[0].z) + uf(d[1].z)) + (uf(d[2].z) + uf(d[3].z));
        s0 += dpp<XOR8>(s0); s1 += dpp<XOR8>(s1);
        s0 = x16x32_sum(s0); s1 = x16x32_sum(s1);
        if (dead) s0 = __builtin_nanf("");
        if (lane < 8) *(LAS f32x2*)(L + L_X2 + wv * 64 + lane * 8) = f32x2{s0, s1};
        STAMP(10);
        WG_BARRIER();
        if (wv == 0 && lane < 8) {
            f32x2 s = {0.f, 0.f};
#pragma unroll
            for (int i = 0; i < NSW; ++i) s += *(const LAS f32x2*)(L + L_X2 + i * 64 + lane * 8);
            const int col = 16 * (w >> 2) + 2 * lane;
            *(GAS unsigned*)(p.out + col) = pk(fmaf(1.f - sc[0], bflo(xv), s[0]), fmaf(1.f - sc[0], bfhi(xv), s[1]));
            if (w == 0 && lane == 0) __builtin_amdgcn_raw_buffer_store_b32(tag, rs, WS_EPOCH, 0, AUX_SC1);
        }
        STAMP(11);
    }
    STAMP_FLUSH();
}

extern "C" void kernel_launch(void* const* d_in, const int* in_sizes, int n_in, void* d_out, int out_size, void* d_ws, size_t ws_size,
                              hipStream_t stream) {
    Params p;
    p.x = (const unsigned short*)d_in[0];
    p.kc = (const unsigned short*)d_in[1];
    p.vc = (const unsigned short*)d_in[2];
    p.wq = (const unsigned short*)d_in[3];
    p.wo = (const unsigned short*)d_in[4];
    p.wr = (const unsigned short*)d_in[6];
    p.wg = (const unsigned short*)d_in[7];
    p.wu = (const unsigned short*)d_in[8];
    p.wd = (const unsigned short*)d_in[9];
    p.gw = (const unsigned short*)d_in[10];
    p.out = (unsigned short*)d_out;
    p.ws = (unsigned char*)d_ws;
    (void)hipFuncSetAttribute((const void*)fused_decode_kernel, hipFuncAttributeMaxDynamicSharedMemorySize, LDS_BYTES);
    hipLaunchKernelGGL(fused_decode_kernel, dim3(NWG), dim3(NTHR), LDS_BYTES, stream, p);
}
```

```cpp
#include <hip/hip_runtime.h>
#include <hip/hip_bf16.h>
#include <stdint.h>

#define GAS __attribute__((address_space(1)))
#define LAS __attribute__((address_space(3)))

typedef unsigned v4u __attribute__((ext_vector_type(4)));
typedef unsigned v2u __attribute__((ext_vector_type(2)));
typedef float f32x4 __attribute__((ext_vector_type(4)));
typedef float f32x2 __attribute__((ext_vector_type(2)));
typedef short bf16x8 __attribute__((ext_vector_type(8)));
typedef short bf16x4 __attribute__((ext_vector_type(4)));
typedef __bf16 bf2 __attribute__((ext_vector_type(2)));

namespace {

constexpr int NWG = 256, NSW = 8, NTHR = (NSW + 1) * 64;
constexpr int DM = 1024, SK = 65536, DH = 128, FF = 3584, NQ = 4096;
constexpr int EARLY = 2;
constexpr int DEPTH = 4;
constexpr int DT = 28;
constexpr int PROG1 = 4;
constexpr int PROG2 = 13;
constexpr int X1FEW = 8;
constexpr int TPR = 4, NROUND = 8;
constexpr int E_END = 64;
constexpr int jmax(int e) {
    const int a = DT - 1 + (e + 2) / 2, b = e + DEPTH + 1;
    const int m = a > b ? a : b;
    return m < E_END - 1 ? m : E_END - 1;
}

constexpr int WS_EPOCH = 0;
constexpr int WS_Q = 4096;
constexpr int WS_Q1 = WS_Q + 2048 * 8;
constexpr int Q1_STRIDE = 32 * 128;
constexpr int WS_ML = WS_Q1 + NWG * Q1_STRIDE;
constexpr int WS_X1 = WS_ML + 32 * 64 * 16;
constexpr int X1_STRIDE = 64 * 128;
constexpr int WS_X2 = WS_X1 + NWG * X1_STRIDE;
constexpr int X2_STRIDE = NWG * 128;
constexpr int WS_H = WS_X2 + 64 * X2_STRIDE;
constexpr int H_STRIDE = 32 * 256;
constexpr int WS_END = WS_H + NWG * H_STRIDE;

constexpr int L_FLAGS = 0;
constexpr int L_HV = 128;
constexpr int L_SC = 192;
constexpr int L_QF = 256;
constexpr int L_ML = L_QF + 2048;
constexpr int L_X1 = L_ML + 512;
constexpr int L_X2 = L_X1 + 64 + 512;
constexpr int L_GU = L_X2 + 512;
constexpr int L_QP = L_GU + 8 * 2 * 448 * 4;
constexpr int L_MRG = L_GU;
constexpr int LDS_BYTES = L_QP + 8 * 512 * 4;
static_assert(8 * 8 * 128 * 4 <= LDS_BYTES - L_GU && LDS_BYTES <= 64 * 1024, "lds");
static_assert(L_MRG % 16 == 0 && L_GU % 16 == 0 && L_QP % 16 == 0, "align");

constexpr unsigned SPIN_CAP = 1u << 18;
constexpr int AUX_SC1 = 16;

struct Params {
    const unsigned short* x;
    const unsigned short* kc;
    const unsigned short* vc;
    const unsigned short* wq;
    const unsigned short* wo;
    const unsigned short* wr;
    const unsigned short* wg;
    const unsigned short* wu;
    const unsigned short* wd;
    const unsigned short* gw;
    unsigned short* out;
    unsigned char* ws;
};

__device__ __forceinline__ v4u ldnt(const unsigned char* p) { return __builtin_nontemporal_load((const GAS v4u*)p); }
__device__ __forceinline__ float bflo(unsigned u) { return __builtin_bit_cast(float, u << 16); }
__device__ __forceinline__ float bfhi(unsigned u) { return __builtin_bit_cast(float, u & 0xffff0000u); }
__device__ __forceinline__ unsigned pk(float lo, float hi) {
    bf2 r = {(__bf16)lo, (__bf16)hi};
    return __builtin_bit_cast(unsigned, r);
}
__device__ __forceinline__ float rbf(float v) { return bflo(pk(v, v)); }
__device__ __forceinline__ unsigned fu(float f) { return __builtin_bit_cast(unsigned, f); }
__device__ __forceinline__ float uf(unsigned u) { return __builtin_bit_cast(float, u); }
template <int CTRL> __device__ __forceinline__ float dpp(float x) {
    return uf((unsigned)__builtin_amdgcn_mov_dpp((int)fu(x), CTRL, 0xf, 0xf, true));
}
constexpr int XOR1 = 0xB1, XOR2 = 0x4E, XOR7 = 0x141, XOR8 = 0x128, ROR4 = 0x124;
__device__ __forceinline__ float swap32_add(float a, float b) {
    auto s = __builtin_amdgcn_permlane32_swap(fu(a), fu(b), false, false);
    return uf(s[0]) + uf(s[1]);
}
__device__ __forceinline__ float swap16_add(float a, float b) {
    auto s = __builtin_amdgcn_permlane16_swap(fu(a), fu(b), false, false);
    return uf(s[0]) + uf(s[1]);
}
__device__ __forceinline__ float x16x32_max(float x) {
    auto s = __builtin_amdgcn_permlane16_swap(fu(x), fu(x), false, false);
    x = fmaxf(uf(s[0]), uf(s[1]));
    auto t = __builtin_amdgcn_permlane32_swap(fu(x), fu(x), false, false);
    return fmaxf(uf(t[0]), uf(t[1]));
}
__device__ __forceinline__ float x16x32_sum(float x) { x = swap16_add(x, x); return swap32_add(x, x); }
__device__ __forceinline__ float row_sum(float x) {
    x += dpp<XOR1>(x); x += dpp<XOR2>(x); x += dpp<XOR7>(x); x += dpp<XOR8>(x); return x;
}
__device__ __forceinline__ float row_max(float x) {
    x = fmaxf(x, dpp<XOR1>(x)); x = fmaxf(x, dpp<XOR2>(x)); x = fmaxf(x, dpp<XOR7>(x)); x = fmaxf(x, dpp<XOR8>(x)); return x;
}
__device__ __forceinline__ float wave_sum(float x) { return x16x32_sum(row_sum(x)); }
__device__ __forceinline__ float wave_max(float x) { return x16x32_max(row_max(x)); }

__device__ __forceinline__ f32x4 mfma32(v4u a, v4u b, f32x4 c) {
    return __builtin_amdgcn_mfma_f32_16x16x32_bf16(__builtin_bit_cast(bf16x8, a), __builtin_bit_cast(bf16x8, b), c, 0, 0, 0);
}
__device__ __forceinline__ f32x4 mfma16(v2u a, v2u b, f32x4 c) {
    return __builtin_amdgcn_mfma_f32_16x16x16bf16_1k(__builtin_bit_cast(bf16x4, a), __builtin_bit_cast(bf16x4, b), c, 0, 0, 0);
}
#define WG_BARRIER() do { asm volatile("s_waitcnt lgkmcnt(0)" ::: "memory"); __builtin_amdgcn_s_barrier(); asm volatile("" ::: "memory"); } while (0)
#define SB() __builtin_amdgcn_sched_barrier(0)
#define THROTTLE() asm volatile("s_waitcnt vmcnt(3)" ::: "memory")
static_assert(DEPTH == 4, "THROTTLE");

__device__ __forceinline__ void fma8(float (&acc)[8], v4u wv, float xv) {
    acc[0] = fmaf(xv, bflo(wv.x), acc[0]); acc[1] = fmaf(xv, bfhi(wv.x), acc[1]);
    acc[2] = fmaf(xv, bflo(wv.y), acc[2]); acc[3] = fmaf(xv, bfhi(wv.y), acc[3]);
    acc[4] = fmaf(xv, bflo(wv.z), acc[4]); acc[5] = fmaf(xv, bfhi(wv.z), acc[5]);
    acc[6] = fmaf(xv, bflo(wv.w), acc[6]); acc[7] = fmaf(xv, bfhi(wv.w), acc[7]);
}
__device__ __forceinline__ v4u ld_sc1(__amdgpu_buffer_rsrc_t rs, int off) { return __builtin_amdgcn_raw_buffer_load_b128(rs, off, 0, AUX_SC1); }
__device__ __forceinline__ void st_sc1(__amdgpu_buffer_rsrc_t rs, int off, v4u v) { __builtin_amdgcn_raw_buffer_store_b128(v, rs, off, 0, AUX_SC1); }

__device__ __forceinline__ void rows_mfma(f32x4 (&acc)[2], v4u wv, unsigned pp, unsigned m0, unsigned m1) {
    const v2u a = {pp & m0, pp & m1};
    acc[0] = mfma16(a, v2u{wv.x, wv.y}, acc[0]);
    acc[1] = mfma16(a, v2u{wv.z, wv.w}, acc[1]);
}
__device__ __forceinline__ unsigned lds_arrive(LAS unsigned char* L, int word, int lane) {
    asm volatile("s_waitcnt lgkmcnt(0)" ::: "memory");
    unsigned old = 0;
    if (lane == 0) old = __hip_atomic_fetch_add((LAS unsigned*)(L + L_FLAGS + 4 * word), 1u, __ATOMIC_RELAXED, __HIP_MEMORY_SCOPE_WORKGROUP);
    old = __builtin_amdgcn_readfirstlane(old);
    asm volatile("" ::: "memory");
    return old;
}
__device__ __forceinline__ void lds_wait_slow(volatile LAS unsigned* flags, int word, unsigned want) {
    for (unsigned spins = 0; flags[word] < want && spins < SPIN_CAP; ++spins) __builtin_amdgcn_s_sleep(8);
    asm volatile("" ::: "memory");
}
__device__ __forceinline__ void lds_wait(volatile LAS unsigned* flags, int word, unsigned want) {
    for (unsigned spins = 0; flags[word] < want && spins < (SPIN_CAP << 4); ++spins) __builtin_amdgcn_s_sleep(1);
    asm volatile("" ::: "memory");
}
__device__ __forceinline__ float qred(f32x4 d) {
    float t = dpp<0x00>(d[0]);
    t = dpp<0x55>(d[1]) + t;
    asm volatile("" : "+v"(t));
    t = dpp<0xAA>(d[2]) + t;
    t = dpp<0xFF>(d[3]) + t;
    return t;
}
template <int T> __device__ __forceinline__ unsigned qbc(unsigned x) {
    return (unsigned)__builtin_amdgcn_mov_dpp((int)x, T * 0x55, 0xf, 0xf, true);
}

}

__global__ void __launch_bounds__(NTHR, 1) fused_decode_kernel(Params p) {
    extern __shared__ __attribute__((aligned(16))) unsigned char smem[];
    LAS unsigned char* L = (LAS unsigned char*)smem;
    volatile LAS unsigned* flags = (volatile LAS unsigned*)(L + L_FLAGS);
    const int tid = threadIdx.x, lane = tid & 63;
    const int wv = __builtin_amdgcn_readfirstlane(tid >> 6);
    const int b = blockIdx.x;
    const int w = (b & 7) * 32 + (b >> 3);
    const int kvh = w >> 6, sp = w & 63;
    const int cb = w & 7, rb = w >> 3;
    const __amdgpu_buffer_rsrc_t rs = __builtin_amdgcn_make_buffer_rsrc((void*)p.ws, 0, WS_END, 0x00020000);

    if (wv == NSW) {
        const unsigned ep = __builtin_amdgcn_raw_buffer_load_b32(rs, WS_EPOCH, 0, AUX_SC1);
        if (lane < 8) flags[lane] = 0u;
        WG_BARRIER();
        const unsigned tag = __builtin_amdgcn_readfirstlane(ep) + 1u;
        if (lane == 0) flags[3] = tag;
        bool dead = false;
        lds_wait(flags, 1, NSW);
#pragma unroll
        for (int it = 0; it < 4; ++it) {
            const int c = it * 128 + lane * 2;
            f32x2 s = {0.f, 0.f};
#pragma unroll
            for (int i = 0; i < NSW; ++i) s += *(const LAS f32x2*)(L + L_QP + (i * 512 + c) * 4);
            st_sc1(rs, WS_Q1 + (cb * 32 + (c >> 4)) * Q1_STRIDE + rb * 128 + (c & 15) * 8, v4u{fu(s[0]), tag, fu(s[1]), tag});
        }
        lds_wait_slow(flags, 4, 1u);
        {
            const int xo = WS_Q1 + w * Q1_STRIDE;
            v4u d[4];
            for (unsigned spins = 0;; ++spins) {
                asm volatile("" ::: "memory");
#pragma unroll
                for (int i = 0; i < 4; ++i) d[i] = ld_sc1(rs, xo + i * 1024 + lane * 16);
                bool ok = true;
#pragma unroll
                for (int i = 0; i < 4; ++i) ok = ok && d[i].y == tag && d[i].w == tag;
                if (__all(ok)) break;
                if (spins > SPIN_CAP) { dead = true; break; }
                __builtin_amdgcn_s_sleep(8);
            }
            float s0 = (uf(d[0].x) + uf(d[1].x)) + (uf(d[2].x) + uf(d[3].x)), s1 = (uf(d[0].z) + uf(d[1].z)) + (uf(d[2].z) + uf(d[3].z));
            s0 += dpp<XOR8>(s0); s1 += dpp<XOR8>(s1);
            s0 = x16x32_sum(s0); s1 = x16x32_sum(s1);
            const unsigned pr = pk(s0, s1);
            const unsigned nx = __builtin_amdgcn_mov_dpp((int)pr, XOR1, 0xf, 0xf, true);
            if (lane < 8 && !(lane & 1) && !dead) st_sc1(rs, WS_Q + (w * 8 + lane) * 8, v4u{pr, tag, nx, tag});
        }
        lds_wait(flags, 2, NSW);
#pragma unroll
        for (int it = 0; it < 7; ++it) {
            const int idx = it * 64 + lane, j = idx / 14, k = idx - 14 * j;
            const int mat = k >= 7 ? 1 : 0, u = 14 * j + 2 * (k - 7 * mat);
            f32x2 s = {0.f, 0.f};
#pragma unroll
            for (int i = 0; i < NSW; ++i) s += *(const LAS f32x2*)(L + L_GU + ((i * 2 + mat) * 448 + u) * 4);
            st_sc1(rs, WS_H + (cb * 32 + j) * H_STRIDE + rb * 256 + k * 16, v4u{fu(s[0]), tag, fu(s[1]), tag});
        }
        lds_wait_slow(flags, 5, 1u);
        {
            const int qoff = WS_Q + (kvh * 512 + ((lane >> 2) & 3) * 64 + (4 * (lane >> 4) + (lane & 3)) * 4) * 8;
            v4u g0, g1, g2, g3;
            for (unsigned spins = 0;; ++spins) {
                asm volatile("" ::: "memory");
                g0 = ld_sc1(rs, qoff); g1 = ld_sc1(rs, qoff + 16); g2 = ld_sc1(rs, qoff + 2048); g3 = ld_sc1(rs, qoff + 2048 + 16);
                const bool ok = g0.y == tag && g0.w == tag && g1.y == tag && g1.w == tag && g2.y == tag && g2.w == tag && g3.y == tag && g3.w == tag;
                if (__all(ok) || dead) break;
                if (spins > SPIN_CAP) { dead = true; break; }
                __builtin_amdgcn_s_sleep(4);
            }
            *(LAS v4u*)(L + L_QF + lane * 16) = v4u{g0.x, g0.z, g1.x, g1.z};
            *(LAS v4u*)(L + L_QF + 1024 + lane * 16) = v4u{g2.x, g2.z, g3.x, g3.z};
            asm volatile("s_waitcnt lgkmcnt(0)" ::: "memory");
            if (lane == 0) flags[0] = dead ? 2u : 1u;
        }
        {
            const GAS v4u* xp = (const GAS v4u*)p.x; const GAS v4u* gp = (const GAS v4u*)p.gw; const GAS v4u* rp = (const GAS v4u*)p.wr;
            float sg = 0.f, sr = 0.f;
#pragma unroll
            for (int i = 0; i < 2; ++i) {
                const v4u xv = xp[lane + 64 * i], gv = gp[lane + 64 * i], rv = rp[lane + 64 * i];
#pragma unroll
                for (int k = 0; k < 4; ++k) {
                    sg += rbf(bflo(xv[k]) * bflo(gv[k])); sg += rbf(bfhi(xv[k]) * bfhi(gv[k]));
                    sr = fmaf(bflo(xv[k]), bflo(rv[k]), sr); sr = fmaf(bfhi(xv[k]), bfhi(rv[k]), sr);
                }
            }
            sg = rbf(wave_sum(sg)); sr = rbf(wave_sum(sr));
            const float g_gate = rbf(1.f / (1.f + __expf(-sg)));
            const float rsg = rbf(1.f / (1.f + __expf(-sr)));
            if (lane == 0) *(LAS f32x2*)(L + L_SC) = f32x2{g_gate, (1.f - g_gate) * rsg};
        }
        lds_wait_slow(flags, 6, 1u);
        {
            const int xo = WS_H + w * H_STRIDE;
            const bool used = (lane & 15) < 14;
            v4u d[8];
            for (unsigned spins = 0;; ++spins) {
                asm volatile("" ::: "memory");
#pragma unroll
                for (int i = 0; i < 8; ++i) d[i] = ld_sc1(rs, xo + i * 1024 + lane * 16);
                bool ok = true;
#pragma unroll
                for (int i = 0; i < 8; ++i) ok = ok && d[i].y == tag && d[i].w == tag;
                if (__all(ok || !used) || dead) break;
                if (spins > SPIN_CAP) { dead = true; break; }
                __builtin_amdgcn_s_sleep(32);
            }
            float s0 = 0.f, s1 = 0.f;
#pragma unroll
            for (int i = 0; i < 8; ++i) { s0 += uf(d[i].x); s1 += uf(d[i].z); }
            s0 = rbf(x16x32_sum(s0)); s1 = rbf(x16x32_sum(s1));
            const float u0 = __shfl(s0, (lane & 15) + 7), u1 = __shfl(s1, (lane & 15) + 7);
            const float a0 = rbf(s0 / (1.f + __expf(-s0))), a1 = rbf(s1 / (1.f + __expf(-s1)));
            float h0 = rbf(a0 * u0), h1 = rbf(a1 * u1);
            if (lane >= 7) { h0 = 0.f; h1 = 0.f; }
            if (dead) h0 = __builtin_nanf("");
            if (lane < 8) *(LAS f32x2*)(L + L_HV + lane * 8) = f32x2{h0, h1};
        }
        WG_BARRIER();
        return;
    }

    const int grow = 32 * rb + 4 * wv;
    const unsigned char* wq_p = (const unsigned char*)p.wq + (size_t)grow * (NQ * 2) + cb * 1024 + lane * 16;
    const size_t kv_base = ((size_t)kvh * SK + (size_t)sp * 1024 + wv * 4) * 256;
    constexpr size_t KV_STEP = 8192;
    const unsigned char* k_p = (const unsigned char*)p.kc + kv_base + ((lane >> 2) & 3) * 256 + (4 * (lane >> 4) + (lane & 3)) * 16;
    const unsigned char* v_p = (const unsigned char*)p.vc + kv_base + lane * 16;
    const int glane = lane < 56 ? lane : 55;
    const size_t goff = (size_t)grow * (FF * 2) + cb * 896 + glane * 16;
    const unsigned char* wg_p = (const unsigned char*)p.wg + goff;
    const unsigned char* wu_p = (const unsigned char*)p.wu + goff;
    const unsigned char* wd_p = (const unsigned char*)p.wd + (size_t)(w * 14) * (DM * 2) + wv * 256 + (lane & 15) * 16;
    const unsigned char* wo_p = (const unsigned char*)p.wo + (size_t)(w * 16 + (lane >> 4)) * (DM * 2) + wv * 256 + (lane & 15) * 16;

    auto issue_w = [&](int i) -> v4u {
        if (i < 4) return ldnt(wq_p + (size_t)i * (NQ * 2));
        if (i < 8) return ldnt(wg_p + (size_t)(i - 4) * (FF * 2));
        if (i < 12) return ldnt(wu_p + (size_t)(i - 8) * (FF * 2));
        if (i < 16) { const int r = 4 * (i - 12) + (lane >> 4); return ldnt(wd_p + (size_t)(r < 14 ? r : 13) * (DM * 2)); }
        return ldnt(wo_p + (size_t)(i - 16) * 4 * (DM * 2));
    };
    auto issue = [&](int e) -> v4u {
        const int r = e / (2 * TPR), k = e % (2 * TPR);
        return k < TPR ? ldnt(k_p + (size_t)(r * TPR + k) * KV_STEP) : ldnt(v_p + (size_t)(r * TPR + k - TPR) * KV_STEP);
    };
    v4u ring[DT], tl[8];
#define ISSUE(j) do { THROTTLE(); ring[(j) % DT] = issue(j); SB(); } while (0)
#define AFTER(e) do { if (jmax(e) > ((e) == 0 ? DT - 1 : jmax((e) - 1))) ISSUE(jmax(e)); } while (0)
#define GET(e) ring[(e) % DT]

    const v2u xg = *(const GAS v2u*)(p.x + grow);
    v4u wt[12];
#pragma unroll
    for (int i = 0; i < 4; ++i) wt[i] = issue_w(i);
    SB();
    WG_BARRIER();
#pragma unroll
    for (int i = 4; i < 4 + EARLY; ++i) wt[i] = issue_w(i);
    SB();
    bool dead = false;
    {
        const float xv[4] = {bflo(xg.x), bfhi(xg.x), bflo(xg.y), bfhi(xg.y)};
        float acc[8] = {0.f, 0.f, 0.f, 0.f, 0.f, 0.f, 0.f, 0.f};
#pragma unroll
        for (int t = 0; t < 4; ++t) fma8(acc, wt[t], xv[t]);
        {
            LAS unsigned char* q = L + L_QP + (wv * 512 + lane * 8) * 4;
            *(LAS f32x4*)(q) = f32x4{acc[0], acc[1], acc[2], acc[3]}; *(LAS f32x4*)(q + 16) = f32x4{acc[4], acc[5], acc[6], acc[7]};
        }
        lds_arrive(L, 1, lane);
        float ag[8] = {0.f, 0.f, 0.f, 0.f, 0.f, 0.f, 0.f, 0.f}, au[8] = {0.f, 0.f, 0.f, 0.f, 0.f, 0.f, 0.f, 0.f};
#pragma unroll
        for (int t = 4 + EARLY; t < 12; ++t) { THROTTLE(); wt[t] = issue_w(t); SB(); }
#pragma unroll
        for (int j = 0; j < 4; ++j) ISSUE(j);
#pragma unroll
        for (int t = 0; t < 4; ++t) { fma8(ag, wt[4 + t], xv[t]); }
#pragma unroll
        for (int t = 0; t < 4; ++t) { fma8(au, wt[8 + t], xv[t]); }
        if (lane < 56) {
            LAS unsigned char* q = L + L_GU + (wv * 2 * 448 + lane * 8) * 4;
            *(LAS f32x4*)(q) = f32x4{ag[0], ag[1], ag[2], ag[3]}; *(LAS f32x4*)(q + 16) = f32x4{ag[4], ag[5], ag[6], ag[7]};
            *(LAS f32x4*)(q + 1792) = f32x4{au[0], au[1], au[2], au[3]}; *(LAS f32x4*)(q + 1792 + 16) = f32x4{au[4], au[5], au[6], au[7]};
        }
        lds_arrive(L, 2, lane);
    }
#pragma unroll
    for (int j = 4; j < DT; ++j) {
        ISSUE(j);
        if (j == PROG1 && wv == 0 && lane == 0) flags[4] = 1u;
        if (j == PROG2 && wv == 0 && lane == 0) flags[5] = 1u;
    }
    lds_wait(flags, 0, 1u);
    if (flags[0] != 1u) dead = true;
    const v4u qf0 = *(const LAS v4u*)(L + L_QF + lane * 16), qf1 = *(const LAS v4u*)(L + L_QF + 1024 + lane * 16);

    constexpr float C2 = 0.088388347648318440f * 1.4426950408889634f;
    const unsigned mk0 = (lane & 3) == 0 ? 0x0000ffffu : (lane & 3) == 1 ? 0xffff0000u : 0u;
    const unsigned mk1 = (lane & 3) == 2 ? 0x0000ffffu : (lane & 3) == 3 ? 0xffff0000u : 0u;
    const int sub = lane & 3;
    f32x4 o00 = {0.f, 0.f, 0.f, 0.f}, o01 = o00, o10 = o00, o11 = o00;
    float m0 = 0.f, m1 = 0.f, l0 = 0.f, l1 = 0.f;
#pragma unroll
    for (int r = 0; r < NROUND; ++r) {
        const int base = r * 2 * TPR;
        const f32x4 z = {0.f, 0.f, 0.f, 0.f};
        f32x4 d0[TPR], d1[TPR];
        float P0, P1;
        d0[0] = mfma32(GET(base + 0), qf0, z); d1[0] = mfma32(GET(base + 0), qf1, z); AFTER(base + 0);
        d0[1] = mfma32(GET(base + 1), qf0, z); d1[1] = mfma32(GET(base + 1), qf1, z); AFTER(base + 1);
        d0[2] = mfma32(GET(base + 2), qf0, z); d1[2] = mfma32(GET(base + 2), qf1, z);
        P0 = qred(d0[0]); P1 = qred(d1[0]);
        AFTER(base + 2);
        d0[3] = mfma32(GET(base + 3), qf0, z); d1[3] = mfma32(GET(base + 3), qf1, z);
        { const float a = qred(d0[1]), c = qred(d1[1]); P0 = sub == 1 ? a : P0; P1 = sub == 1 ? c : P1; }
        AFTER(base + 3);
        { const float a = qred(d0[2]), c = qred(d1[2]); P0 = sub == 2 ? a : P0; P1 = sub == 2 ? c : P1; }
        { const float a = qred(d0[3]), c = qred(d1[3]); P0 = sub == 3 ? a : P0; P1 = sub == 3 ? c : P1; }
        float t0 = fmaxf(P0, dpp<XOR1>(P0)), t1 = fmaxf(P1, dpp<XOR1>(P1));
        t0 = fmaxf(t0, dpp<XOR2>(t0)); t1 = fmaxf(t1, dpp<XOR2>(t1));
        t0 = x16x32_max(t0) * C2; t1 = x16x32_max(t1) * C2;
        if (r == 0) { m0 = t0; m1 = t1; }
        else if (__any(t0 > m0 || t1 > m1)) {
            const float n0 = fmaxf(m0, t0), n1 = fmaxf(m1, t1);
            const float a0 = __builtin_amdgcn_exp2f(m0 - n0), a1 = __builtin_amdgcn_exp2f(m1 - n1);
            m0 = n0; m1 = n1; l0 *= a0; l1 *= a1;
            const float b0 = __shfl(a0, (lane >> 4) * 20), b1 = __shfl(a1, (lane >> 4) * 20);
            o00 *= b0; o01 *= b0; o10 *= b1; o11 *= b1;
        }
        const float p0 = __builtin_amdgcn_exp2f(fmaf(P0, C2, -m0)), p1 = __builtin_amdgcn_exp2f(fmaf(P1, C2, -m1));
        l0 += p0; l1 += p1;
        const unsigned pp0 = pk(p0, p0), pp1 = pk(p1, p1);
#define PV(t) do { \
            const unsigned b0_ = qbc<t>(pp0), b1_ = qbc<t>(pp1); \
            const v2u a0_ = {b0_ & mk0, b0_ & mk1}, a1_ = {b1_ & mk0, b1_ & mk1}; \
            const v4u vf_ = GET(base + TPR + (t)); \
            o00 = mfma16(a0_, v2u{vf_.x, vf_.y}, o00); o01 = mfma16(a0_, v2u{vf_.z, vf_.w}, o01); \
            o10 = mfma16(a1_, v2u{vf_.x, vf_.y}, o10); o11 = mfma16(a1_, v2u{vf_.z, vf_.w}, o11); \
            AFTER(base + TPR + (t)); } while (0)
        PV(0); PV(1); PV(2); PV(3);
        if (r == NROUND / 2 - 1 && wv == 0 && lane == 0) flags[6] = 1u;
    }
    l0 += dpp<XOR1>(l0); l1 += dpp<XOR1>(l1); l0 += dpp<XOR2>(l0); l1 += dpp<XOR2>(l1);
    l0 = x16x32_sum(l0); l1 = x16x32_sum(l1);
    {
        LAS unsigned char* mb = L + L_MRG + wv * 4096 + (lane >> 4) * 512 + (lane & 15) * 32;
        *(LAS f32x4*)(mb) = o00; *(LAS f32x4*)(mb + 16) = o01;
        *(LAS f32x4*)(mb + 2048) = o10; *(LAS f32x4*)(mb + 2048 + 16) = o11;
        if ((lane & 0x33) == 0) {
            LAS unsigned char* q = L + L_ML + wv * 64 + (lane >> 2) * 8;
            *(LAS f32x2*)(q) = f32x2{m0, dead ? __builtin_nanf("") : l0}; *(LAS f32x2*)(q + 32) = f32x2{m1, l1};
        }
    }
    WG_BARRIER();
    const unsigned tag = flags[3];
    {
        f32x2 ml[NSW];
#pragma unroll
        for (int i = 0; i < NSW; ++i) ml[i] = *(const LAS f32x2*)(L + L_ML + i * 64 + wv * 8);
        float M = ml[0][0];
#pragma unroll
        for (int i = 1; i < NSW; ++i) M = fmaxf(M, ml[i][0]);
        float Ls = 0.f, a0 = 0.f, a1 = 0.f;
#pragma unroll
        for (int i = 0; i < NSW; ++i) {
            const float wt_ = __builtin_amdgcn_exp2f(ml[i][0] - M);
            const f32x2 o = *(const LAS f32x2*)(L + L_MRG + i * 4096 + wv * 512 + lane * 8);
            Ls = fmaf(wt_, ml[i][1], Ls); a0 = fmaf(wt_, o[0], a0); a1 = fmaf(wt_, o[1], a1);
        }
        st_sc1(rs, WS_X1 + (kvh * 64 + wv * 8 + (lane >> 3)) * X1_STRIDE + sp * 128 + (lane & 7) * 16, v4u{fu(a0), tag, fu(a1), tag});
        if (lane == 0) st_sc1(rs, WS_ML + ((kvh * 8 + wv) * 64 + sp) * 16, v4u{fu(M), tag, fu(Ls), tag});
    }
    const f32x2 sc = *(const LAS f32x2*)(L + L_SC);
    f32x4 acc[2] = {{0.f, 0.f, 0.f, 0.f}, {0.f, 0.f, 0.f, 0.f}};
    {
        const int xo = WS_X1 + w * X1_STRIDE + wv * 1024 + lane * 16;
        const int mo = WS_ML + ((w >> 3) * 64 + wv * 8 + (lane >> 3)) * 16;
        v4u ml = ld_sc1(rs, mo), d = ld_sc1(rs, xo);
        SB();
#pragma unroll
        for (int t = 0; t < 8; ++t) tl[t] = issue_w(12 + t);
        SB();
        {
            unsigned long long got = __ballot(ml.y == tag && ml.w == tag);
            bool have = got == ~0ull && __all(d.y == tag && d.w == tag);
            for (unsigned spins = 0; !have; ++spins) {
                asm volatile("" ::: "memory");
                if (got != ~0ull) ml = ld_sc1(rs, mo);
                if (__builtin_popcountll(~got) <= X1FEW) d = ld_sc1(rs, xo);
                got = __ballot(ml.y == tag && ml.w == tag);
                have = got == ~0ull && __all(d.y == tag && d.w == tag);
                if (spins > SPIN_CAP) { dead = true; break; }
            }
        }
        const float m = uf(ml.x);
        float M = fmaxf(m, dpp<XOR8>(m)); M = x16x32_max(M);
        const float wt_ = __builtin_amdgcn_exp2f(m - M);
        float Ls = wt_ * uf(ml.z), o0 = wt_ * uf(d.x), o1 = wt_ * uf(d.z);
        Ls += dpp<XOR8>(Ls); o0 += dpp<XOR8>(o0); o1 += dpp<XOR8>(o1);
        Ls = x16x32_sum(Ls); o0 = x16x32_sum(o0); o1 = x16x32_sum(o1);
        if (lane < 8) {
            *(LAS float*)(L + L_X1 + 64 + ((2 * lane) * 8 + wv) * 4) = o0;
            *(LAS float*)(L + L_X1 + 64 + ((2 * lane + 1) * 8 + wv) * 4) = o1;
            if (lane == 0) *(LAS f32x2*)(L + L_X1 + wv * 8) = f32x2{M, dead ? __builtin_nanf("") : Ls};
        }
#pragma unroll
        for (int t = 0; t < 4; ++t) {
            const float h = *(const LAS float*)(L + L_HV + (4 * t + (lane >> 4)) * 4) * sc[1];
            rows_mfma(acc, tl[t], pk(h, h), mk0, mk1);
        }
    }
    WG_BARRIER();
    {
        f32x2 ml[NSW];
#pragma unroll
        for (int i = 0; i < NSW; ++i) ml[i] = *(const LAS f32x2*)(L + L_X1 + i * 8);
        float M = ml[0][0];
#pragma unroll
        for (int i = 1; i < NSW; ++i) M = fmaxf(M, ml[i][0]);
        float wt_[NSW], Ls = 0.f;
#pragma unroll
        for (int i = 0; i < NSW; ++i) { wt_[i] = __builtin_amdgcn_exp2f(ml[i][0] - M); Ls = fmaf(wt_[i], ml[i][1], Ls); }
        const float inv = 1.f / Ls;
#pragma unroll
        for (int t = 0; t < 4; ++t) {
            const LAS unsigned char* q = L + L_X1 + 64 + (4 * t + (lane >> 4)) * 32;
            const f32x4 u = *(const LAS f32x4*)(q), v = *(const LAS f32x4*)(q + 16);
            float o = wt_[0] * u[0];
            o = fmaf(wt_[1], u[1], o); o = fmaf(wt_[2], u[2], o); o = fmaf(wt_[3], u[3], o);
            o = fmaf(wt_[4], v[0], o); o = fmaf(wt_[5], v[1], o); o = fmaf(wt_[6], v[2], o); o = fmaf(wt_[7], v[3], o);
            o = rbf(o * inv) * sc[0];
            rows_mfma(acc, tl[4 + t], pk(o, o), mk0, mk1);
        }
        const int g2 = lane >> 4, c = lane & 15;
        const f32x4 a = (g2 & 2) ? acc[1] : acc[0];
        const float v0 = (g2 & 1) ? a[2] : a[0], v1 = (g2 & 1) ? a[3] : a[1];
        const int cons = 8 * wv + (c >> 1);
        st_sc1(rs, WS_X2 + cons * X2_STRIDE + w * 128 + ((c & 1) * 4 + g2) * 16, v4u{fu(v0), tag, fu(v1), tag});
    }
    if ((w & 3) == 0) {
        const int xo = WS_X2 + (w >> 2) * X2_STRIDE + wv * 4096 + lane * 16;
        const unsigned xv = *(const GAS unsigned*)(p.x + 16 * (w >> 2) + 2 * (lane & 7));
        v4u d[4];
#pragma unroll
        for (int i = 0; i < 4; ++i) d[i] = ld_sc1(rs, xo + i * 1024);
        for (unsigned spins = 0;; ++spins) {
            unsigned pend = 0u;
#pragma unroll
            for (int i = 0; i < 4; ++i) if (!__all(d[i].y == tag && d[i].w == tag)) pend |= 1u << i;
            if (pend == 0u) break;
            if (spins > SPIN_CAP) { dead = true; break; }
            asm volatile("" ::: "memory");
#pragma unroll
            for (int i = 0; i < 4; ++i) if ((pend >> i) & 1u) d[i] = ld_sc1(rs, xo + i * 1024);
        }
        float s0 = (uf(d[0].x) + uf(d[1].x)) + (uf(d[2].x) + uf(d[3].x)), s1 = (uf(d[0].z) + uf(d[1].z)) + (uf(d[2].z) + uf(d[3].z));
        s0 += dpp<XOR8>(s0); s1 += dpp<XOR8>(s1);
        s0 = x16x32_sum(s0); s1 = x16x32_sum(s1);
        if (dead) s0 = __builtin_nanf("");
        if (lane < 8) *(LAS f32x2*)(L + L_X2 + wv * 64 + lane * 8) = f32x2{s0, s1};
        WG_BARRIER();
        if (wv == 0 && lane < 8) {
            f32x2 s = {0.f, 0.f};
#pragma unroll
            for (int i = 0; i < NSW; ++i) s += *(const LAS f32x2*)(L + L_X2 + i * 64 + lane * 8);
            const int col = 16 * (w >> 2) + 2 * lane;
            *(GAS unsigned*)(p.out + col) = pk(fmaf(1.f - sc[0], bflo(xv), s[0]), fmaf(1.f - sc[0], bfhi(xv), s[1]));
            if (w == 0 && lane == 0) __builtin_amdgcn_raw_buffer_store_b32(tag, rs, WS_EPOCH, 0, AUX_SC1);
        }
    }
}

extern "C" void kernel_launch(void* const* d_in, const int* in_sizes, int n_in, void* d_out, int out_size, void* d_ws, size_t ws_size,
                              hipStream_t stream) {
    Params p;
    p.x = (const unsigned short*)d_in[0];
    p.kc = (const unsigned short*)d_in[1];
    p.vc = (const unsigned short*)d_in[2];
    p.wq = (const unsigned short*)d_in[3];
    p.wo = (const unsigned short*)d_in[4];
    p.wr = (const unsigned short*)d_in[6];
    p.wg = (const unsigned short*)d_in[7];
    p.wu = (const unsigned short*)d_in[8];
    p.wd = (const unsigned short*)d_in[9];
    p.gw = (const unsigned short*)d_in[10];
    p.out = (unsigned short*)d_out;
    p.ws = (unsigned char*)d_ws;
    hipLaunchKernelGGL(fused_decode_kernel, dim3(NWG), dim3(NTHR), LDS_BYTES, stream, p);
}
```
